```python
import torch

def _softmax(x, axis=-1):
    return torch.softmax(x, dim=axis)
def _gelu(x):
    return torch.nn.functional.gelu(x, approximate="tanh")
def _silu(x):
    return torch.nn.functional.silu(x)
def _sigmoid(x):
    return torch.sigmoid(x)
def _layernorm(x, axis=-1, eps=1e-5):
    m = x.mean(dim=axis, keepdim=True)
    v = x.var(dim=axis, keepdim=True, unbiased=False)
    return (x - m) / torch.sqrt(v + eps)
def _scatter_add(tgt, idx, src, axis=0):
    return tgt.clone().index_add_(axis, idx, src)
def _segment_sum(src, seg, n):
    out = torch.zeros((n,) + src.shape[1:], dtype=src.dtype)
    return out.index_add_(0, seg, src)
def _pad(x, pw):
    flat = tuple(v for pair in reversed(pw) for v in pair)
    return torch.nn.functional.pad(x, flat)

kernel_name = 'sequential_preLN__ssm_conv_h3__moe_arch_mod__prompt'

def setup_inputs():

    _w = (lambda a: (lambda t: t.to(torch.bfloat16) if t.is_floating_point() else t)(torch.as_tensor(a)))
    def _A_setup():
        B, S, D = 1, 2048, 640
        ks = 4
        return {
            "q": _w((lambda *s: torch.randn(*s, dtype=torch.bfloat16) * 0.05)(B, S, D)),
            "k": _w((lambda *s: torch.randn(*s, dtype=torch.bfloat16) * 0.2)(B, S, D)),
            "v": _w((lambda *s: torch.randn(*s, dtype=torch.bfloat16) * 0.2)(B, S, D)),
            "shift_k": _w((lambda *s: torch.randn(*s, dtype=torch.bfloat16) * 0.2)(D, ks) * 0.5),
            "ssm_k": _w((lambda *s: torch.randn(*s, dtype=torch.bfloat16) * 0.8)(D, S) * (1.0 / S ** 0.5)),
            "ssm_D": _w((lambda *s: torch.randn(*s, dtype=torch.bfloat16) * 0.2)(D)),
        }
    _A_in = _A_setup()

    def _B_setup():
        B, S, D, F, C = 1, 2048, 640, 1728, 8
        return {
            "x": _w((lambda *s: torch.randn(*s, dtype=torch.bfloat16) * 0.05)(B, S, D)),
            "w_r": _w((lambda *s: torch.randn(*s, dtype=torch.bfloat16) * 0.05)(D)),
            "w_gate": _w((lambda *s: torch.randn(*s, dtype=torch.bfloat16) * 0.05)(D, F)),
            "w_up": _w((lambda *s: torch.randn(*s, dtype=torch.bfloat16) * 0.05)(D, F)),
            "w_down": _w((lambda *s: torch.randn(*s, dtype=torch.bfloat16) * 0.05)(F, D)),
        }
    _B_in = _B_setup()

    return {
        "A_q": _A_in["q"],
        "A_k": _A_in["k"],
        "A_v": _A_in["v"],
        "A_shift_k": _A_in["shift_k"],
        "A_ssm_k": _A_in["ssm_k"],
        "A_ssm_D": _A_in["ssm_D"],
        "B_x": _B_in["x"],
        "B_w_r": _B_in["w_r"],
        "B_w_gate": _B_in["w_gate"],
        "B_w_up": _B_in["w_up"],
        "B_w_down": _B_in["w_down"],
    }

def reference(A_q, A_k, A_v, A_shift_k, A_ssm_k, A_ssm_D, B_x, B_w_r, B_w_gate, B_w_up, B_w_down):
    def _A_ref(q, k, v, shift_k, ssm_k, ssm_D):
        B, S, D = q.shape
        ks = shift_k.shape[-1]
        k_pad = _pad(k, ((0, 0), (ks - 1, 0), (0, 0)))
        k_shift = torch.zeros_like(k)
        for t in range(ks):
            k_shift = k_shift + k_pad[:, t:t + S, :] * shift_k[:, ks - 1 - t][None, None, :]
        kv = k_shift * v
        L = 2 * S
        kv_pad = _pad(kv, ((0, 0), (0, S), (0, 0)))
        kf_pad = _pad(ssm_k, ((0, 0), (0, S)))
        kv_f = (lambda x, axes=(-2,-1): torch.fft.rfft2(x.float(), dim=axes))(kv_pad[:, None, :, :], axes=(1, 2))
        kf_f = (lambda x, axes=(-2,-1): torch.fft.rfft2(x.float(), dim=axes))(kf_pad[:, None, :], axes=(1, 2))
        kf_f = (lambda x, *d: x.permute(*d))(kf_f, 1, 2, 0)
        y_f = kv_f * kf_f[None, :, :, :]
        y = (lambda x, s, axes=(-2,-1): torch.fft.irfft2(x, s=s, dim=axes).to(torch.bfloat16))(y_f, (1, L), axes=(1, 2))[:, 0, :S, :]
        y = y + kv * ssm_D[None, None, :]
        return q * y

    def _B_ref(x, w_r, w_gate, w_up, w_down):
        B, S, D = x.shape
        C = 8
        r = x @ w_r
        idx = torch.argsort(-r, axis=-1)[:, :C]
        idx_e = (lambda x, n, axis: torch.repeat_interleave(x, n, dim=axis))(idx.reshape(B, C, 1), D, axis=2)
        x_sel = (lambda x, i, axis: torch.take_along_dim(x, i, dim=axis))(x, idx_e, axis=1)
        g = _silu(x_sel @ w_gate)
        u = x_sel @ w_up
        y = (g * u) @ w_down
        r_sel = _sigmoid((lambda x, i, axis: torch.take_along_dim(x, i, dim=axis))(r, idx, axis=1))
        y = y * r_sel.reshape(B, C, 1)
        b_off = (torch.arange(B) * S).reshape(B, 1)
        flat_idx = (idx + b_off).reshape(B * C)
        zero = (x * 0.0).reshape(B * S, D)
        upd = _scatter_add(zero, flat_idx, y.reshape(B * C, D), axis=0)
        return x + upd.reshape(B, S, D)

    x = A_q
    h = x + _A_ref(_layernorm(x, axis=-1), A_k, A_v, A_shift_k, A_ssm_k, A_ssm_D)
    h2 = h + _B_ref(_layernorm(h, axis=-1), B_w_r, B_w_gate, B_w_up, B_w_down)
    return h2
```

```cpp
#include <hip/hip_runtime.h>
#include <hip/hip_bf16.h>

typedef unsigned short u16;
typedef unsigned int u32;
typedef __attribute__((ext_vector_type(8))) short bf16x8;
typedef __attribute__((ext_vector_type(4))) short s16x4;
typedef __attribute__((ext_vector_type(4))) float f32x4;
typedef __attribute__((ext_vector_type(2))) float f32x2;
typedef __attribute__((ext_vector_type(2))) __bf16 bfx2;
typedef __attribute__((ext_vector_type(4))) unsigned u32x4;
typedef __attribute__((ext_vector_type(2))) unsigned u32x2;

constexpr int S = 2048, D = 640, F = 1728, NSEL = 8;

__device__ __forceinline__ u32 pk2(float a, float b) {
    f32x2 v = {a, b};
    bfx2 r = __builtin_convertvector(v, bfx2);
    return __builtin_bit_cast(u32, r);
}
__device__ __forceinline__ float lo2f(u32 p) { return __uint_as_float(p << 16); }
__device__ __forceinline__ float hi2f(u32 p) { return __uint_as_float(p & 0xffff0000u); }
__device__ __forceinline__ float rbf(float a) { return lo2f(pk2(a, 0.f)); }

__device__ __forceinline__ float wave_sum(float v) {
#pragma unroll
    for (int o = 32; o >= 1; o >>= 1) v += __shfl_xor(v, o, 64);
    return v;
}
__device__ __forceinline__ u32 wave_max_u32(u32 v) {
#pragma unroll
    for (int o = 32; o >= 1; o >>= 1) { u32 w = (u32)__shfl_xor((int)v, o, 64); v = v > w ? v : w; }
    return v;
}

__global__ __launch_bounds__(320) void k0_kv(const u16* __restrict__ k, const u16* __restrict__ v,
                                             const u16* __restrict__ shift_k, u16* __restrict__ uT) {
    const int b = blockIdx.x;
    const int tb = (b & 7) * 32 + (b >> 3);
    const int t0 = tb * 8;
    const int c = threadIdx.x;
    u32 kk[11], vv[8];
#pragma unroll
    for (int i = 0; i < 11; ++i) {
        int t = t0 - 3 + i;
        kk[i] = (t >= 0) ? *(const u32*)(k + (size_t)t * D + 2 * c) : 0u;
    }
#pragma unroll
    for (int i = 0; i < 8; ++i) vv[i] = *(const u32*)(v + (size_t)(t0 + i) * D + 2 * c);
    const u32x4 sw = *(const u32x4*)(shift_k + 8 * c);
    const float a0 = lo2f(sw.x), a1 = hi2f(sw.x), a2 = lo2f(sw.y), a3 = hi2f(sw.y);
    const float b0 = lo2f(sw.z), b1 = hi2f(sw.z), b2 = lo2f(sw.w), b3 = hi2f(sw.w);
    float ua[8], ub[8];
#pragma unroll
    for (int i = 0; i < 8; ++i) {
        u32 p0 = pk2(lo2f(kk[i]) * a3, hi2f(kk[i]) * b3);
        u32 p1 = pk2(lo2f(kk[i + 1]) * a2, hi2f(kk[i + 1]) * b2);
        u32 p2 = pk2(lo2f(kk[i + 2]) * a1, hi2f(kk[i + 2]) * b1);
        u32 p3 = pk2(lo2f(kk[i + 3]) * a0, hi2f(kk[i + 3]) * b0);
        u32 s = pk2(lo2f(p0) + lo2f(p1), hi2f(p0) + hi2f(p1));
        s = pk2(lo2f(s) + lo2f(p2), hi2f(s) + hi2f(p2));
        s = pk2(lo2f(s) + lo2f(p3), hi2f(s) + hi2f(p3));
        ua[i] = lo2f(s) * lo2f(vv[i]);
        ub[i] = hi2f(s) * hi2f(vv[i]);
    }
    u32x4 oa, ob;
    oa.x = pk2(ua[7], ua[6]); oa.y = pk2(ua[5], ua[4]); oa.z = pk2(ua[3], ua[2]); oa.w = pk2(ua[1], ua[0]);
    ob.x = pk2(ub[7], ub[6]); ob.y = pk2(ub[5], ub[4]); ob.z = pk2(ub[3], ub[2]); ob.w = pk2(ub[1], ub[0]);
    *(u32x4*)(uT + (size_t)(2 * c) * S + t0) = oa;
    *(u32x4*)(uT + (size_t)(2 * c + 1) * S + t0) = ob;
}

constexpr int UPAD = 256;
constexpr int KCS = 2080;

__global__ __launch_bounds__(64) void k1_conv(const u16* __restrict__ uT, const u16* __restrict__ ssm_k,
                                              const u16* __restrict__ ssm_D, u16* __restrict__ y2T) {
    __shared__ __attribute__((aligned(16))) u16 lds[UPAD + S + 4 * KCS];
    u16* uL = lds;
    u16* kL = lds + UPAD + S;
    const int d = blockIdx.x;
    const int lane = threadIdx.x;
    const u16* urow = uT + (size_t)d * S;
    const u16* krow = ssm_k + (size_t)d * S;
    u32x4 ur[4];
#pragma unroll
    for (int i = 0; i < 4; ++i) ur[i] = *(const u32x4*)(urow + (i * 64 + lane) * 8);
    u32x4 kd[5];
    u32x2 ke[5];
#pragma unroll
    for (int i = 0; i < 5; ++i) {
        int Y = i * 64 + lane;
        int bb = Y - 2;
        u32x4 z4 = {0u, 0u, 0u, 0u};
        u32x2 z2 = {0u, 0u};
        kd[i] = (bb >= 0 && bb < 256) ? *(const u32x4*)(krow + bb * 8) : z4;
        ke[i] = (bb + 1 >= 0 && bb + 1 < 256) ? *(const u32x2*)(krow + bb * 8 + 8) : z2;
    }
    const float Dv = __uint_as_float(((u32)ssm_D[d]) << 16);
#pragma unroll
    for (int i = 0; i < 4; ++i) *(u32x4*)(uL + UPAD + (i * 64 + lane) * 8) = ur[i];
    if (lane < 32) {
        u32x4 z4 = {0u, 0u, 0u, 0u};
        *(u32x4*)(uL + lane * 8) = z4;
    }
#pragma unroll
    for (int i = 0; i < 5; ++i) {
        int Y = i * 64 + lane;
        if (Y < 258) {
            u32 d0 = kd[i].x, d1 = kd[i].y, d2 = kd[i].z, d3 = kd[i].w, d4 = ke[i].x, d5 = ke[i].y;
            u32 s0 = __builtin_amdgcn_alignbit(d1, d0, 16);
            u32 s1 = __builtin_amdgcn_alignbit(d2, d1, 16);
            u32 s2 = __builtin_amdgcn_alignbit(d3, d2, 16);
            u32 s3 = __builtin_amdgcn_alignbit(d4, d3, 16);
            u32 s4 = __builtin_amdgcn_alignbit(d5, d4, 16);
            u32x4 c0 = {d0, d1, d2, d3}, c1 = {s0, s1, s2, s3}, c2 = {d1, d2, d3, d4}, c3 = {s1, s2, s3, s4};
            *(u32x4*)(kL + 0 * KCS + Y * 8) = c0;
            *(u32x4*)(kL + 1 * KCS + Y * 8) = c1;
            *(u32x4*)(kL + 2 * KCS + Y * 8) = c2;
            *(u32x4*)(kL + 3 * KCS + Y * 8) = c3;
        }
    }
    __syncthreads();
    const int li = lane & 15, g = lane >> 4;
    const u16* aBase = kL + ((li + 1) & 3) * KCS + ((li + 1) & ~3) + 8 * g;
    const u16* bBase = uL + UPAD + 16 * li + 8 - 8 * g;
    f32x4 acc[8];
#pragma unroll
    for (int c = 0; c < 8; ++c) acc[c] = (f32x4){0.f, 0.f, 0.f, 0.f};
#pragma unroll 2
    for (int rho = 0; rho < 8; ++rho) {
        bf16x8 af[8], bf[8];
#pragma unroll
        for (int n = 0; n < 8; ++n) {
            const u16* p = aBase + 32 * (8 * n + rho);
            s16x4 lo = *(const s16x4*)(p);
            s16x4 hi = *(const s16x4*)(p + 4);
            af[n] = __builtin_shufflevector(lo, hi, 0, 1, 2, 3, 4, 5, 6, 7);
        }
#pragma unroll
        for (int bq = 0; bq < 8; ++bq) bf[bq] = *(const bf16x8*)(bBase + 32 * (8 * bq - rho));
#pragma unroll
        for (int n = 0; n < 8; ++n)
#pragma unroll
            for (int bq = 0; bq + n < 8; ++bq)
                acc[n + bq] = __builtin_amdgcn_mfma_f32_16x16x32_bf16(af[n], bf[bq], acc[n + bq], 0, 0, 0);
    }
    u16* yrow = y2T + (size_t)d * S;
#pragma unroll
    for (int c = 0; c < 8; ++c) {
        const int tb = 256 * c + 16 * li + 8 * (g >> 1);
        u32x2 uu = *(const u32x2*)(uL + UPAD + tb + 4 * (1 - (g & 1)));
        float u3 = lo2f(uu.x), u2 = hi2f(uu.x), u1 = lo2f(uu.y), u0 = hi2f(uu.y);
        u32 y01 = pk2(acc[c][0], acc[c][1]);
        u32 y23 = pk2(acc[c][2], acc[c][3]);
        u32 w01 = pk2(u0 * Dv, u1 * Dv);
        u32 w23 = pk2(u2 * Dv, u3 * Dv);
        u32x2 o;
        o.x = pk2(lo2f(y01) + lo2f(w01), hi2f(y01) + hi2f(w01));
        o.y = pk2(lo2f(y23) + lo2f(w23), hi2f(y23) + hi2f(w23));
        *(u32x2*)(yrow + 256 * c + 16 * li + 4 * g) = o;
    }
}

__device__ __forceinline__ void unpack10(const u32x4& a, u32 b, float* f) {
    f[0] = lo2f(a.x); f[1] = hi2f(a.x); f[2] = lo2f(a.y); f[3] = hi2f(a.y);
    f[4] = lo2f(a.z); f[5] = hi2f(a.z); f[6] = lo2f(a.w); f[7] = hi2f(a.w);
    f[8] = lo2f(b); f[9] = hi2f(b);
}
__device__ __forceinline__ void ln_stats(const float* x, float& m_bf, float& s_bf) {
    float s = 0.f;
#pragma unroll
    for (int i = 0; i < 10; ++i) s += x[i];
    s = wave_sum(s);
    const float mean = s * (1.0f / D);
    float q = 0.f;
#pragma unroll
    for (int i = 0; i < 10; ++i) { float dd = x[i] - mean; q += dd * dd; }
    q = wave_sum(q);
    const float var = q * (1.0f / D);
    m_bf = rbf(mean);
    float vb = rbf(var);
    float ve = rbf(vb + 1e-5f);
    s_bf = rbf(sqrtf(ve));
}

__global__ __launch_bounds__(512) void k2_row(const u16* __restrict__ q, const u16* __restrict__ y2T,
                                              const u16* __restrict__ w_r, u16* __restrict__ out,
                                              u16* __restrict__ hbuf, float* __restrict__ stats,
                                              u32* __restrict__ keys) {
    __shared__ __attribute__((aligned(16))) u32 yt[8][320];
    const int b = blockIdx.x;
    const int tb = (b & 7) * 32 + (b >> 3);
    const int t0 = tb * 8;
    const int tid = threadIdx.x;
    const int w = tid >> 6, lane = tid & 63;
    const int t = t0 + w;
    u32x4 ya = {0u, 0u, 0u, 0u}, yb = {0u, 0u, 0u, 0u};
    if (tid < 320) {
        ya = *(const u32x4*)(y2T + (size_t)(2 * tid) * S + t0);
        yb = *(const u32x4*)(y2T + (size_t)(2 * tid + 1) * S + t0);
    }
    const u32x4 xa = *(const u32x4*)(q + (size_t)t * D + 8 * lane);
    const u32 xb = *(const u32*)(q + (size_t)t * D + 512 + 2 * lane);
    const u32x4 wa = *(const u32x4*)(w_r + 8 * lane);
    const u32 wb = *(const u32*)(w_r + 512 + 2 * lane);
    if (tid < 320) {
        yt[0][tid] = (ya.x & 0xffffu) | (yb.x << 16);
        yt[1][tid] = (ya.x >> 16) | (yb.x & 0xffff0000u);
        yt[2][tid] = (ya.y & 0xffffu) | (yb.y << 16);
        yt[3][tid] = (ya.y >> 16) | (yb.y & 0xffff0000u);
        yt[4][tid] = (ya.z & 0xffffu) | (yb.z << 16);
        yt[5][tid] = (ya.z >> 16) | (yb.z & 0xffff0000u);
        yt[6][tid] = (ya.w & 0xffffu) | (yb.w << 16);
        yt[7][tid] = (ya.w >> 16) | (yb.w & 0xffff0000u);
    }
    float x[10];
    unpack10(xa, xb, x);
    float m1, s1;
    ln_stats(x, m1, s1);
    const float r1 = 1.0f / s1;
    __syncthreads();
    const u32x4 y4 = *(const u32x4*)(&yt[w][4 * lane]);
    const u32 y1 = yt[w][256 + lane];
    float y[10];
    unpack10(y4, y1, y);
    float h[10];
#pragma unroll
    for (int i = 0; i < 10; i += 2) {
        u32 dd = pk2(x[i] - m1, x[i + 1] - m1);
        u32 l1 = pk2(lo2f(dd) * r1, hi2f(dd) * r1);
        u32 a = pk2(lo2f(l1) * y[i], hi2f(l1) * y[i + 1]);
        u32 hh = pk2(x[i] + lo2f(a), x[i + 1] + hi2f(a));
        h[i] = lo2f(hh);
        h[i + 1] = hi2f(hh);
    }
    float m2, s2;
    ln_stats(h, m2, s2);
    const float r2 = 1.0f / s2;
    float wr[10];
    unpack10(wa, wb, wr);
    float racc = 0.f;
    u32 hp[5], op[5];
#pragma unroll
    for (int i = 0; i < 10; i += 2) {
        u32 dd = pk2(h[i] - m2, h[i + 1] - m2);
        u32 l2 = pk2(lo2f(dd) * r2, hi2f(dd) * r2);
        racc += lo2f(l2) * wr[i] + hi2f(l2) * wr[i + 1];
        hp[i >> 1] = pk2(h[i], h[i + 1]);
        op[i >> 1] = pk2(h[i] + lo2f(l2), h[i + 1] + hi2f(l2));
    }
    racc = wave_sum(racc);
    *(u32x4*)(out + (size_t)t * D + 8 * lane) = (u32x4){op[0], op[1], op[2], op[3]};
    *(u32*)(out + (size_t)t * D + 512 + 2 * lane) = op[4];
    *(u32x4*)(hbuf + (size_t)t * D + 8 * lane) = (u32x4){hp[0], hp[1], hp[2], hp[3]};
    *(u32*)(hbuf + (size_t)t * D + 512 + 2 * lane) = hp[4];
    if (lane == 0) {
        u32 rb = pk2(racc, 0.f) & 0xffffu;
        u32 ord = (rb & 0x8000u) ? (rb ^ 0xffffu) : (rb ^ 0x8000u);
        keys[t] = (ord << 16) | (u32)(0xffff - t);
        stats[2 * t] = m2;
        stats[2 * t + 1] = r2;
    }
}

typedef __attribute__((address_space(3))) s16x4* lds_s16x4_ptr;
__device__ __forceinline__ s16x4 tr_read(const u16* p) {
    return __builtin_amdgcn_ds_read_tr16_b64_v4i16((lds_s16x4_ptr)p);
}
__device__ __forceinline__ float key_r(u32 key) {
    u32 ord = key >> 16;
    u32 rb = (ord & 0x8000u) ? (ord ^ 0x8000u) : (ord ^ 0xffffu);
    return __uint_as_float(rb << 16);
}

constexpr int XS = 648;
__global__ __launch_bounds__(256) void k3_gateup(const u32* __restrict__ keys, const u16* __restrict__ hbuf,
                                                 const float* __restrict__ stats, const u16* __restrict__ w_gate,
                                                 const u16* __restrict__ w_up, u16* __restrict__ gu,
                                                 u32* __restrict__ selkeys) {
    __shared__ __attribute__((aligned(16))) u16 wg[D * 16];
    __shared__ __attribute__((aligned(16))) u16 wu[D * 16];
    __shared__ __attribute__((aligned(16))) u16 xs[8 * XS];
    __shared__ __attribute__((aligned(16))) float red[4][2][8][16];
    __shared__ u32 cand[32];
    __shared__ u32 sel[8];
    const int b = blockIdx.x;
    const int sl = (b & 7) * 14 + (b >> 3);
    const int x = b & 7, r = b >> 3;
    const int slice = (x < 4) ? (x * 14 + r) : (56 + (x - 4) * 13 + r);
    (void)sl;
    const int f0 = slice * 16;
    const int tid = threadIdx.x, w = tid >> 6, lane = tid & 63;
    u32x4 lg[5], lu[5];
#pragma unroll
    for (int i = 0; i < 5; ++i) {
        int ch = i * 256 + tid;
        int row = ch >> 1, hf = ch & 1;
        lg[i] = *(const u32x4*)(w_gate + (size_t)row * F + f0 + 8 * hf);
        lu[i] = *(const u32x4*)(w_up + (size_t)row * F + f0 + 8 * hf);
    }
    u32 a[8];
    {
        u32x4 k0 = *(const u32x4*)(keys + tid * 8);
        u32x4 k1 = *(const u32x4*)(keys + tid * 8 + 4);
        a[0] = k0.x; a[1] = k0.y; a[2] = k0.z; a[3] = k0.w; a[4] = k1.x; a[5] = k1.y; a[6] = k1.z; a[7] = k1.w;
    }
#define CE(i, j) { u32 hi_ = a[i] > a[j] ? a[i] : a[j]; u32 lo_ = a[i] > a[j] ? a[j] : a[i]; a[i] = hi_; a[j] = lo_; }
    CE(0, 1) CE(2, 3) CE(4, 5) CE(6, 7)
    CE(0, 2) CE(1, 3) CE(4, 6) CE(5, 7)
    CE(1, 2) CE(5, 6) CE(0, 4) CE(3, 7)
    CE(1, 5) CE(2, 6)
    CE(1, 4) CE(3, 6)
    CE(2, 4) CE(3, 5)
    CE(3, 4)
#undef CE
    u32 mine = 0;
#pragma unroll
    for (int rd = 0; rd < 8; ++rd) {
        u32 m = wave_max_u32(a[0]);
        bool win = (a[0] == m);
#pragma unroll
        for (int i = 0; i < 7; ++i) a[i] = win ? a[i + 1] : a[i];
        a[7] = win ? 0u : a[7];
        if (lane == rd) mine = m;
    }
    if (lane < 8) cand[w * 8 + lane] = mine;
#pragma unroll
    for (int i = 0; i < 5; ++i) {
        int ch = i * 256 + tid;
        *(u32x4*)(wg + ch * 8) = lg[i];
        *(u32x4*)(wu + ch * 8) = lu[i];
    }
    __syncthreads();
    {
        u32 c = (lane < 32) ? cand[lane] : 0u;
        u32 mysel = 0;
#pragma unroll
        for (int rd = 0; rd < 8; ++rd) {
            u32 m = wave_max_u32(c);
            c = (c == m) ? 0u : c;
            if (lane == rd) mysel = m;
        }
#pragma unroll
        for (int j = 0; j < 2; ++j) {
            const int tok = 2 * w + j;
            u32 key = (u32)__shfl((int)mysel, tok, 64);
            const int t = 0xffff - (int)(key & 0xffffu);
            const u32x4 ha = *(const u32x4*)(hbuf + (size_t)t * D + 8 * lane);
            const u32 hb = *(const u32*)(hbuf + (size_t)t * D + 512 + 2 * lane);
            const float m2 = stats[2 * t], r2 = stats[2 * t + 1];
            float h[10];
            unpack10(ha, hb, h);
            u32 l2[5];
#pragma unroll
            for (int i = 0; i < 10; i += 2) {
                u32 dd = pk2(h[i] - m2, h[i + 1] - m2);
                l2[i >> 1] = pk2(lo2f(dd) * r2, hi2f(dd) * r2);
            }
            *(u32x4*)(xs + tok * XS + 8 * lane) = (u32x4){l2[0], l2[1], l2[2], l2[3]};
            *(u32*)(xs + tok * XS + 512 + 2 * lane) = l2[4];
        }
        if (b == 0 && w == 0 && lane < 8) selkeys[lane] = mysel;
    }
    __syncthreads();
    {
        const int li = lane & 15, g = lane >> 4;
        f32x4 ag = {0.f, 0.f, 0.f, 0.f}, au = {0.f, 0.f, 0.f, 0.f};
        const int q4 = li >> 2, p4 = li & 3;
#pragma unroll
        for (int s = 0; s < 5; ++s) {
            const int k0 = (5 * w + s) * 32;
            bf16x8 af = *(const bf16x8*)(xs + (li & 7) * XS + k0 + 8 * g);
            const int ro = (k0 + 8 * g + q4) * 16 + 4 * p4;
            s16x4 g0 = tr_read(wg + ro), g1 = tr_read(wg + ro + 64);
            s16x4 u0 = tr_read(wu + ro), u1 = tr_read(wu + ro + 64);
            bf16x8 bg = __builtin_shufflevector(g0, g1, 0, 1, 2, 3, 4, 5, 6, 7);
            bf16x8 bu = __builtin_shufflevector(u0, u1, 0, 1, 2, 3, 4, 5, 6, 7);
            ag = __builtin_amdgcn_mfma_f32_16x16x32_bf16(af, bg, ag, 0, 0, 0);
            au = __builtin_amdgcn_mfma_f32_16x16x32_bf16(af, bu, au, 0, 0, 0);
        }
        if (g < 2) {
#pragma unroll
            for (int rr = 0; rr < 4; ++rr) {
                red[w][0][4 * g + rr][li] = ag[rr];
                red[w][1][4 * g + rr][li] = au[rr];
            }
        }
    }
    __syncthreads();
    if (tid < 128) {
        const int tok = tid >> 4, col = tid & 15;
        float gs = red[0][0][tok][col] + red[1][0][tok][col] + red[2][0][tok][col] + red[3][0][tok][col];
        float us = red[0][1][tok][col] + red[1][1][tok][col] + red[2][1][tok][col] + red[3][1][tok][col];
        float gb = rbf(gs), ub = rbf(us);
        float sg = rbf(gb / (1.0f + __expf(-gb)));
        u32 o = pk2(sg * ub, 0.f);
        gu[tok * F + f0 + col] = (u16)(o & 0xffffu);
    }
}

constexpr int GS = 1736;
__global__ __launch_bounds__(512) void k4_down(const u16* __restrict__ gu, const u16* __restrict__ w_down,
                                               const u32* __restrict__ selkeys, const u16* __restrict__ hbuf,
                                               const float* __restrict__ stats, u16* __restrict__ out) {
    __shared__ __attribute__((aligned(16))) u16 wd[F * 16];
    __shared__ __attribute__((aligned(16))) u16 gs[8 * GS];
    __shared__ __attribute__((aligned(16))) float red[8][8][16];
    const int b = blockIdx.x;
    const int slice = (b & 7) * 5 + (b >> 3);
    const int d0 = slice * 16;
    const int tid = threadIdx.x, w = tid >> 6, lane = tid & 63;
    u32x4 lw[7];
#pragma unroll
    for (int i = 0; i < 7; ++i) {
        int ch = i * 512 + tid;
        if (ch < 2 * F) {
            int row = ch >> 1, hf = ch & 1;
            lw[i] = *(const u32x4*)(w_down + (size_t)row * D + d0 + 8 * hf);
        }
    }
    u32x4 lg[4];
#pragma unroll
    for (int i = 0; i < 4; ++i) {
        int ch = i * 512 + tid;
        if (ch < 1728) lg[i] = *(const u32x4*)(gu + ch * 8);
    }
    float hv = 0.f, l2v = 0.f, rs = 0.f;
    int trow = 0;
    if (tid < 128) {
        const int tok = tid >> 4, col = tid & 15;
        u32 key = selkeys[tok];
        trow = 0xffff - (int)(key & 0xffffu);
        float rv = key_r(key);
        rs = rbf(1.0f / (1.0f + __expf(-rv)));
        hv = __uint_as_float(((u32)hbuf[(size_t)trow * D + d0 + col]) << 16);
        float m2 = stats[2 * trow], r2 = stats[2 * trow + 1];
        l2v = rbf(rbf(hv - m2) * r2);
    }
#pragma unroll
    for (int i = 0; i < 7; ++i) {
        int ch = i * 512 + tid;
        if (ch < 2 * F) *(u32x4*)(wd + ch * 8) = lw[i];
    }
#pragma unroll
    for (int i = 0; i < 4; ++i) {
        int ch = i * 512 + tid;
        if (ch < 1728) {
            int tok = ch / 216, cc = ch - tok * 216;
            *(u32x4*)(gs + tok * GS + cc * 8) = lg[i];
        }
    }
    __syncthreads();
    {
        const int li = lane & 15, g = lane >> 4;
        const int q4 = li >> 2, p4 = li & 3;
        f32x4 acc = {0.f, 0.f, 0.f, 0.f};
        for (int s = w; s < 54; s += 8) {
            const int k0 = s * 32;
            bf16x8 af = *(const bf16x8*)(gs + (li & 7) * GS + k0 + 8 * g);
            const int ro = (k0 + 8 * g + q4) * 16 + 4 * p4;
            s16x4 b0 = tr_read(wd + ro), b1 = tr_read(wd + ro + 64);
            bf16x8 bf = __builtin_shufflevector(b0, b1, 0, 1, 2, 3, 4, 5, 6, 7);
            acc = __builtin_amdgcn_mfma_f32_16x16x32_bf16(af, bf, acc, 0, 0, 0);
        }
        if (g < 2) {
#pragma unroll
            for (int rr = 0; rr < 4; ++rr) red[w][4 * g + rr][li] = acc[rr];
        }
    }
    __syncthreads();
    if (tid < 128) {
        const int tok = tid >> 4, col = tid & 15;
        float s = 0.f;
#pragma unroll
        for (int i = 0; i < 8; ++i) s += red[i][tok][col];
        float y = rbf(s);
        y = rbf(y * rs);
        float o1 = rbf(l2v + y);
        u32 o = pk2(hv + o1, 0.f);
        out[(size_t)trow * D + d0 + col] = (u16)(o & 0xffffu);
    }
}

extern "C" void kernel_launch(void* const* d_in, const int* in_sizes, int n_in, void* d_out, int out_size,
                              void* d_ws, size_t ws_size, hipStream_t stream) {
    const u16* q = (const u16*)d_in[0];
    const u16* k = (const u16*)d_in[1];
    const u16* v = (const u16*)d_in[2];
    const u16* shift_k = (const u16*)d_in[3];
    const u16* ssm_k = (const u16*)d_in[4];
    const u16* ssm_D = (const u16*)d_in[5];
    const u16* w_r = (const u16*)d_in[7];
    const u16* w_gate = (const u16*)d_in[8];
    const u16* w_up = (const u16*)d_in[9];
    const u16* w_down = (const u16*)d_in[10];
    u16* out = (u16*)d_out;
    char* ws = (char*)d_ws;
    const size_t TS = (size_t)S * D * 2;
    u16* uT = (u16*)(ws);
    u16* y2T = (u16*)(ws + TS);
    u16* hbuf = (u16*)(ws + 2 * TS);
    float* stats = (float*)(ws + 3 * TS);
    u32* keys = (u32*)(ws + 3 * TS + 16384);
    u32* selkeys = (u32*)(ws + 3 * TS + 16384 + 8192);
    u16* gu = (u16*)(ws + 3 * TS + 16384 + 8192 + 256);
    k0_kv<<<256, 320, 0, stream>>>(k, v, shift_k, uT);
    k1_conv<<<640, 64, 0, stream>>>(uT, ssm_k, ssm_D, y2T);
    k2_row<<<256, 512, 0, stream>>>(q, y2T, w_r, out, hbuf, stats, keys);
    k3_gateup<<<108, 256, 0, stream>>>(keys, hbuf, stats, w_gate, w_up, gu, selkeys);
    k4_down<<<40, 512, 0, stream>>>(gu, w_down, selkeys, hbuf, stats, out);
}
```

```cpp
#include <hip/hip_runtime.h>
#include <hip/hip_bf16.h>

typedef unsigned short u16;
typedef unsigned int u32;
typedef __attribute__((ext_vector_type(8))) short bf16x8;
typedef __attribute__((ext_vector_type(4))) short s16x4;
typedef __attribute__((ext_vector_type(4))) float f32x4;
typedef __attribute__((ext_vector_type(2))) float f32x2;
typedef __attribute__((ext_vector_type(2))) __bf16 bfx2;
typedef __attribute__((ext_vector_type(4))) unsigned u32x4;
typedef __attribute__((ext_vector_type(2))) unsigned u32x2;

constexpr int S = 2048, D = 640, F = 1728, NSEL = 8;

#define STAMP(kid, i)

__device__ __forceinline__ u32 pk2(float a, float b) {
    f32x2 v = {a, b};
    bfx2 r = __builtin_convertvector(v, bfx2);
    return __builtin_bit_cast(u32, r);
}
__device__ __forceinline__ float lo2f(u32 p) { return __uint_as_float(p << 16); }
__device__ __forceinline__ float hi2f(u32 p) { return __uint_as_float(p & 0xffff0000u); }
__device__ __forceinline__ float rbf(float a) { return lo2f(pk2(a, 0.f)); }

template <int CTRL, int RM> __device__ __forceinline__ int dpp_i(int v) {
    return __builtin_amdgcn_update_dpp(0, v, CTRL, RM, 0xf, true);
}
__device__ __forceinline__ float wave_sum(float v) {
    v += __int_as_float(dpp_i<0x111, 0xf>(__float_as_int(v)));
    v += __int_as_float(dpp_i<0x112, 0xf>(__float_as_int(v)));
    v += __int_as_float(dpp_i<0x114, 0xf>(__float_as_int(v)));
    v += __int_as_float(dpp_i<0x118, 0xf>(__float_as_int(v)));
    v += __int_as_float(dpp_i<0x142, 0xa>(__float_as_int(v)));
    v += __int_as_float(dpp_i<0x143, 0xc>(__float_as_int(v)));
    return __int_as_float(__builtin_amdgcn_readlane(__float_as_int(v), 63));
}
__device__ __forceinline__ u32 umax_(u32 a, u32 b) { return a > b ? a : b; }
__device__ __forceinline__ u32 wave_max_u32(u32 v) {
    v = umax_(v, (u32)dpp_i<0x111, 0xf>((int)v));
    v = umax_(v, (u32)dpp_i<0x112, 0xf>((int)v));
    v = umax_(v, (u32)dpp_i<0x114, 0xf>((int)v));
    v = umax_(v, (u32)dpp_i<0x118, 0xf>((int)v));
    v = umax_(v, (u32)dpp_i<0x142, 0xa>((int)v));
    v = umax_(v, (u32)dpp_i<0x143, 0xc>((int)v));
    return (u32)__builtin_amdgcn_readlane((int)v, 63);
}
#define KEEP4(val_) asm volatile("" ::"v"((val_).x), "v"((val_).y), "v"((val_).z), "v"((val_).w))

__global__ __launch_bounds__(320) void k0_kv(const u16* __restrict__ k, const u16* __restrict__ v,
                                             const u16* __restrict__ shift_k, u16* __restrict__ uT,
                                             const u16* __restrict__ pf0, const u16* __restrict__ pf1) {
    const int b = blockIdx.x;
    const int rb = (b & 7) * 64 + (b >> 3);
    const int t0 = rb * 4;
    const int c = threadIdx.x;
    STAMP(0, 0);
    u32 kk[7], vv[4];
#pragma unroll
    for (int i = 0; i < 7; ++i) {
        int t = t0 - 3 + i;
        kk[i] = (t >= 0) ? *(const u32*)(k + (size_t)t * D + 2 * c) : 0u;
    }
#pragma unroll
    for (int i = 0; i < 4; ++i) vv[i] = *(const u32*)(v + (size_t)(t0 + i) * D + 2 * c);
    const u32x4 sw = *(const u32x4*)(shift_k + 8 * c);
    const u32x4 pa = *(const u32x4*)(pf0 + (size_t)(b * 320 + c) * 8);
    const u32x4 pb = *(const u32x4*)(pf1 + (size_t)(b * 320 + c) * 8);
    const float a0 = lo2f(sw.x), a1 = hi2f(sw.x), a2 = lo2f(sw.y), a3 = hi2f(sw.y);
    const float b0 = lo2f(sw.z), b1 = hi2f(sw.z), b2 = lo2f(sw.w), b3 = hi2f(sw.w);
    float ua[4], ub[4];
#pragma unroll
    for (int i = 0; i < 4; ++i) {
        u32 p0 = pk2(lo2f(kk[i]) * a3, hi2f(kk[i]) * b3);
        u32 p1 = pk2(lo2f(kk[i + 1]) * a2, hi2f(kk[i + 1]) * b2);
        u32 p2 = pk2(lo2f(kk[i + 2]) * a1, hi2f(kk[i + 2]) * b1);
        u32 p3 = pk2(lo2f(kk[i + 3]) * a0, hi2f(kk[i + 3]) * b0);
        u32 s = pk2(lo2f(p0) + lo2f(p1), hi2f(p0) + hi2f(p1));
        s = pk2(lo2f(s) + lo2f(p2), hi2f(s) + hi2f(p2));
        s = pk2(lo2f(s) + lo2f(p3), hi2f(s) + hi2f(p3));
        ua[i] = lo2f(s) * lo2f(vv[i]);
        ub[i] = hi2f(s) * hi2f(vv[i]);
    }
    u32x2 oa, ob;
    oa.x = pk2(ua[3], ua[2]); oa.y = pk2(ua[1], ua[0]);
    ob.x = pk2(ub[3], ub[2]); ob.y = pk2(ub[1], ub[0]);
    STAMP(0, 1);
    *(u32x2*)(uT + (size_t)(2 * c) * S + (t0 ^ 4)) = oa;
    *(u32x2*)(uT + (size_t)(2 * c + 1) * S + (t0 ^ 4)) = ob;
    KEEP4(pa);
    KEEP4(pb);
    STAMP(0, 2);
}

constexpr int UPAD = 256;
constexpr int KCS = 2080;
constexpr int WCH = 138240;

__global__ __launch_bounds__(256) void k1_conv(const u16* __restrict__ uT, const u16* __restrict__ ssm_k,
                                               const u16* __restrict__ ssm_D, u16* __restrict__ y2T,
                                               const u16* __restrict__ pf0, const u16* __restrict__ pf1,
                                               const u16* __restrict__ pf2) {
    __shared__ __attribute__((aligned(16))) u16 lds[UPAD + S + 16384];
    u16* uL = lds;
    u16* kL = lds + UPAD + S;
    f32x4* part = (f32x4*)(lds + UPAD + S);
    const int d = blockIdx.x;
    const int tid = threadIdx.x;
    const int w = tid >> 6, lane = tid & 63;
    const u16* urow = uT + (size_t)d * S;
    const u16* krow = ssm_k + (size_t)d * S;
    STAMP(1, 0);
    const u32x4 ur = *(const u32x4*)(urow + tid * 8);
    const u32x4 kd = *(const u32x4*)(krow + tid * 8);
    u32x2 ke = {0u, 0u};
    if (tid < 255) ke = *(const u32x2*)(krow + tid * 8 + 8);
    const float Dv = __uint_as_float(((u32)ssm_D[d]) << 16);
    const int gid = d * 256 + tid;
    u32x4 pa = {0u, 0u, 0u, 0u}, pb = pa, pc = pa;
    if (gid < WCH) {
        pa = *(const u32x4*)(pf0 + (size_t)gid * 8);
        pb = *(const u32x4*)(pf1 + (size_t)gid * 8);
        pc = *(const u32x4*)(pf2 + (size_t)gid * 8);
    }
    *(u32x4*)(uL + UPAD + tid * 8) = ur;
    const u32x4 z4 = {0u, 0u, 0u, 0u};
    if (tid < 32) *(u32x4*)(uL + tid * 8) = z4;
    {
        u32 d0 = kd.x, d1 = kd.y, d2 = kd.z, d3 = kd.w, d4 = ke.x, d5 = ke.y;
        u32 s0 = __builtin_amdgcn_alignbit(d1, d0, 16);
        u32 s1 = __builtin_amdgcn_alignbit(d2, d1, 16);
        u32 s2 = __builtin_amdgcn_alignbit(d3, d2, 16);
        u32 s3 = __builtin_amdgcn_alignbit(d4, d3, 16);
        u32 s4 = __builtin_amdgcn_alignbit(d5, d4, 16);
        u32x4 c0 = {d0, d1, d2, d3}, c1 = {s0, s1, s2, s3}, c2 = {d1, d2, d3, d4}, c3 = {s1, s2, s3, s4};
        const int Y = tid + 2;
        *(u32x4*)(kL + 0 * KCS + Y * 8) = c0;
        *(u32x4*)(kL + 1 * KCS + Y * 8) = c1;
        *(u32x4*)(kL + 2 * KCS + Y * 8) = c2;
        *(u32x4*)(kL + 3 * KCS + Y * 8) = c3;
        if (tid == 0) {
            u32x4 e1 = {0u, 0u, 0u, d0 << 16}, e2 = {0u, 0u, 0u, d0}, e3 = {0u, 0u, d0 << 16, s0};
            *(u32x4*)(kL + 0 * KCS + 0) = z4; *(u32x4*)(kL + 0 * KCS + 8) = z4;
            *(u32x4*)(kL + 1 * KCS + 0) = z4; *(u32x4*)(kL + 1 * KCS + 8) = e1;
            *(u32x4*)(kL + 2 * KCS + 0) = z4; *(u32x4*)(kL + 2 * KCS + 8) = e2;
            *(u32x4*)(kL + 3 * KCS + 0) = z4; *(u32x4*)(kL + 3 * KCS + 8) = e3;
        }
    }
    __syncthreads();
    STAMP(1, 1);
    const int li = lane & 15, g = lane >> 4;
    const u16* aBase = kL + ((li + 1) & 3) * KCS + ((li + 1) & ~3) + 8 * g + 64 * w;
    const u16* bBase = uL + UPAD + 16 * li + 8 - 8 * g - 64 * w;
    f32x4 acc[8];
#pragma unroll
    for (int c = 0; c < 8; ++c) acc[c] = (f32x4){0.f, 0.f, 0.f, 0.f};
    bf16x8 af[2][8], bf[2][8];
#pragma unroll
    for (int r = 0; r < 2; ++r) {
#pragma unroll
        for (int n = 0; n < 8; ++n) {
            const u16* p = aBase + 32 * (8 * n + r);
            s16x4 lo = *(const s16x4*)(p);
            s16x4 hi = *(const s16x4*)(p + 4);
            af[r][n] = __builtin_shufflevector(lo, hi, 0, 1, 2, 3, 4, 5, 6, 7);
            bf[r][n] = *(const bf16x8*)(bBase + 32 * (8 * n - r));
        }
    }
#pragma unroll
    for (int r = 0; r < 2; ++r)
#pragma unroll
        for (int n = 0; n < 8; ++n)
#pragma unroll
            for (int bq = 0; bq + n < 8; ++bq)
                acc[n + bq] = __builtin_amdgcn_mfma_f32_16x16x32_bf16(af[r][n], bf[r][bq], acc[n + bq], 0, 0, 0);
    STAMP(1, 2);
    __syncthreads();
#pragma unroll
    for (int c = 0; c < 8; ++c) part[(w * 8 + c) * 64 + lane] = acc[c];
    __syncthreads();
    STAMP(1, 3);
    u16* yrow = y2T + (size_t)d * S;
#pragma unroll
    for (int j = 0; j < 2; ++j) {
        const int c = 2 * w + j;
        f32x4 a0 = part[(0 * 8 + c) * 64 + lane], a1 = part[(1 * 8 + c) * 64 + lane];
        f32x4 a2 = part[(2 * 8 + c) * 64 + lane], a3 = part[(3 * 8 + c) * 64 + lane];
        f32x4 a = (a0 + a1) + (a2 + a3);
        const int tb = 256 * c + 16 * li + 8 * (g >> 1);
        u32x2 uu = *(const u32x2*)(uL + UPAD + tb + 4 * (1 - (g & 1)));
        float u3 = lo2f(uu.x), u2 = hi2f(uu.x), u1 = lo2f(uu.y), u0 = hi2f(uu.y);
        u32 y01 = pk2(a[0], a[1]);
        u32 y23 = pk2(a[2], a[3]);
        u32 w01 = pk2(u0 * Dv, u1 * Dv);
        u32 w23 = pk2(u2 * Dv, u3 * Dv);
        u32x2 o;
        o.x = pk2(lo2f(y01) + lo2f(w01), hi2f(y01) + hi2f(w01));
        o.y = pk2(lo2f(y23) + lo2f(w23), hi2f(y23) + hi2f(w23));
        *(u32x2*)(yrow + 256 * c + 16 * li + 4 * g) = o;
    }
    KEEP4(pa);
    KEEP4(pb);
    KEEP4(pc);
    STAMP(1, 4);
}

__device__ __forceinline__ void unpack10(const u32x4& a, u32 b, float* f) {
    f[0] = lo2f(a.x); f[1] = hi2f(a.x); f[2] = lo2f(a.y); f[3] = hi2f(a.y);
    f[4] = lo2f(a.z); f[5] = hi2f(a.z); f[6] = lo2f(a.w); f[7] = hi2f(a.w);
    f[8] = lo2f(b); f[9] = hi2f(b);
}
__device__ __forceinline__ void ln_stats(const float* x, float& m_bf, float& s_bf) {
    float s = ((x[0] + x[1]) + (x[2] + x[3])) + ((x[4] + x[5]) + (x[6] + x[7])) + (x[8] + x[9]);
    s = wave_sum(s);
    const float mean = s * (1.0f / D);
    float q0 = 0.f, q1 = 0.f;
#pragma unroll
    for (int i = 0; i < 10; i += 2) {
        float d0 = x[i] - mean, d1 = x[i + 1] - mean;
        q0 += d0 * d0;
        q1 += d1 * d1;
    }
    float q = wave_sum(q0 + q1);
    const float var = q * (1.0f / D);
    m_bf = rbf(mean);
    float vb = rbf(var);
    float ve = rbf(vb + 1e-5f);
    s_bf = rbf(sqrtf(ve));
}

__global__ __launch_bounds__(512) void k2_row(const u16* __restrict__ q, const u16* __restrict__ y2T,
                                              const u16* __restrict__ w_r, u16* __restrict__ out,
                                              u16* __restrict__ hbuf, float* __restrict__ stats,
                                              u32* __restrict__ keys) {
    __shared__ __attribute__((aligned(16))) u32 yt[8][320];
    const int b = blockIdx.x;
    const int tb = (b & 7) * 32 + (b >> 3);
    const int t0 = tb * 8;
    const int tid = threadIdx.x;
    const int w = tid >> 6, lane = tid & 63;
    const int t = t0 + w;
    STAMP(2, 0);
    const u32x4 xa = *(const u32x4*)(q + (size_t)t * D + 8 * lane);
    const u32 xb = *(const u32*)(q + (size_t)t * D + 512 + 2 * lane);
    u32x4 ya = {0u, 0u, 0u, 0u}, yb = {0u, 0u, 0u, 0u};
    if (tid < 320) {
        ya = *(const u32x4*)(y2T + (size_t)(2 * tid) * S + t0);
        yb = *(const u32x4*)(y2T + (size_t)(2 * tid + 1) * S + t0);
    }
    const u32x4 wa = *(const u32x4*)(w_r + 8 * lane);
    const u32 wb = *(const u32*)(w_r + 512 + 2 * lane);
    float x[10];
    unpack10(xa, xb, x);
    float m1, s1;
    ln_stats(x, m1, s1);
    const float r1 = 1.0f / s1;
    STAMP(2, 1);
    if (tid < 320) {
        yt[0][tid] = __builtin_amdgcn_perm(yb.x, ya.x, 0x05040100);
        yt[1][tid] = __builtin_amdgcn_perm(yb.x, ya.x, 0x07060302);
        yt[2][tid] = __builtin_amdgcn_perm(yb.y, ya.y, 0x05040100);
        yt[3][tid] = __builtin_amdgcn_perm(yb.y, ya.y, 0x07060302);
        yt[4][tid] = __builtin_amdgcn_perm(yb.z, ya.z, 0x05040100);
        yt[5][tid] = __builtin_amdgcn_perm(yb.z, ya.z, 0x07060302);
        yt[6][tid] = __builtin_amdgcn_perm(yb.w, ya.w, 0x05040100);
        yt[7][tid] = __builtin_amdgcn_perm(yb.w, ya.w, 0x07060302);
    }
    u32 l1p[5];
#pragma unroll
    for (int i = 0; i < 10; i += 2) {
        u32 dd = pk2(x[i] - m1, x[i + 1] - m1);
        l1p[i >> 1] = pk2(lo2f(dd) * r1, hi2f(dd) * r1);
    }
    __syncthreads();
    STAMP(2, 2);
    const u32x4 y4 = *(const u32x4*)(&yt[w][4 * lane]);
    const u32 y1 = yt[w][256 + lane];
    float y[10];
    unpack10(y4, y1, y);
    float h[10];
    u32 hp[5], op[5];
#pragma unroll
    for (int i = 0; i < 10; i += 2) {
        u32 l1 = l1p[i >> 1];
        u32 a = pk2(lo2f(l1) * y[i], hi2f(l1) * y[i + 1]);
        u32 hh = pk2(x[i] + lo2f(a), x[i + 1] + hi2f(a));
        hp[i >> 1] = hh;
        h[i] = lo2f(hh);
        h[i + 1] = hi2f(hh);
    }
    *(u32x4*)(hbuf + (size_t)t * D + 8 * lane) = (u32x4){hp[0], hp[1], hp[2], hp[3]};
    *(u32*)(hbuf + (size_t)t * D + 512 + 2 * lane) = hp[4];
    float m2, s2;
    ln_stats(h, m2, s2);
    const float r2 = 1.0f / s2;
    float wr[10];
    unpack10(wa, wb, wr);
    float racc0 = 0.f, racc1 = 0.f;
#pragma unroll
    for (int i = 0; i < 10; i += 2) {
        u32 dd = pk2(h[i] - m2, h[i + 1] - m2);
        u32 l2 = pk2(lo2f(dd) * r2, hi2f(dd) * r2);
        racc0 += lo2f(l2) * wr[i];
        racc1 += hi2f(l2) * wr[i + 1];
        op[i >> 1] = pk2(h[i] + lo2f(l2), h[i + 1] + hi2f(l2));
    }
    *(u32x4*)(out + (size_t)t * D + 8 * lane) = (u32x4){op[0], op[1], op[2], op[3]};
    *(u32*)(out + (size_t)t * D + 512 + 2 * lane) = op[4];
    const float racc = wave_sum(racc0 + racc1);
    STAMP(2, 3);
    if (lane == 0) {
        u32 rb = pk2(racc, 0.f) & 0xffffu;
        u32 ord = (rb & 0x8000u) ? (rb ^ 0xffffu) : (rb ^ 0x8000u);
        keys[t] = (ord << 16) | (u32)(0xffff - t);
        *(f32x2*)(stats + 2 * t) = (f32x2){m2, r2};
    }
    STAMP(2, 4);
}

typedef __attribute__((address_space(3))) s16x4* lds_s16x4_ptr;
__device__ __forceinline__ s16x4 tr_read(const u16* p) {
    return __builtin_amdgcn_ds_read_tr16_b64_v4i16((lds_s16x4_ptr)p);
}
__device__ __forceinline__ float key_r(u32 key) {
    u32 ord = key >> 16;
    u32 rb = (ord & 0x8000u) ? (ord ^ 0x8000u) : (ord ^ 0xffffu);
    return __uint_as_float(rb << 16);
}

constexpr int XS = 648;
__global__ __launch_bounds__(256) void k3_gateup(const u32* __restrict__ keys, const u16* __restrict__ hbuf,
                                                 const float* __restrict__ stats, const u16* __restrict__ w_gate,
                                                 const u16* __restrict__ w_up, u16* __restrict__ gu,
                                                 u32* __restrict__ selkeys, u16* __restrict__ selh,
                                                 u16* __restrict__ sell2) {
    __shared__ __attribute__((aligned(16))) u16 wg[D * 16];
    __shared__ __attribute__((aligned(16))) u16 wu[D * 16];
    __shared__ __attribute__((aligned(16))) u16 xs[8 * XS];
    __shared__ __attribute__((aligned(16))) float red[4][2][8][16];
    __shared__ u32 cand[32];
    const int b = blockIdx.x;
    const int x = b & 7, r = b >> 3;
    const int slice = (x < 4) ? (x * 14 + r) : (56 + (x - 4) * 13 + r);
    const int f0 = slice * 16;
    const int tid = threadIdx.x, w = tid >> 6, lane = tid & 63;
    STAMP(3, 0);
    u32 a[8];
    {
        u32x4 k0 = *(const u32x4*)(keys + tid * 8);
        u32x4 k1 = *(const u32x4*)(keys + tid * 8 + 4);
        a[0] = k0.x; a[1] = k0.y; a[2] = k0.z; a[3] = k0.w; a[4] = k1.x; a[5] = k1.y; a[6] = k1.z; a[7] = k1.w;
    }
    u32x4 lg[5], lu[5];
#pragma unroll
    for (int i = 0; i < 5; ++i) {
        int ch = i * 256 + tid;
        int row = ch >> 1, hf = ch & 1;
        lg[i] = *(const u32x4*)(w_gate + (size_t)row * F + f0 + 8 * hf);
        lu[i] = *(const u32x4*)(w_up + (size_t)row * F + f0 + 8 * hf);
    }
#define CE(i, j) { u32 hi_ = a[i] > a[j] ? a[i] : a[j]; u32 lo_ = a[i] > a[j] ? a[j] : a[i]; a[i] = hi_; a[j] = lo_; }
    CE(0, 1) CE(2, 3) CE(4, 5) CE(6, 7)
    CE(0, 2) CE(1, 3) CE(4, 6) CE(5, 7)
    CE(1, 2) CE(5, 6) CE(0, 4) CE(3, 7)
    CE(1, 5) CE(2, 6)
    CE(1, 4) CE(3, 6)
    CE(2, 4) CE(3, 5)
    CE(3, 4)
#undef CE
    u32 mine = 0;
#pragma unroll
    for (int rd = 0; rd < 8; ++rd) {
        u32 m = wave_max_u32(a[0]);
        bool win = (a[0] == m);
#pragma unroll
        for (int i = 0; i < 7 - rd; ++i) a[i] = win ? a[i + 1] : a[i];
        if (lane == rd) mine = m;
    }
    if (lane < 8) cand[w * 8 + lane] = mine;
    STAMP(3, 1);
    __syncthreads();
    STAMP(3, 2);
    {
        u32 c = (lane < 32) ? cand[lane] : 0u;
        u32 k0 = 0, k1 = 0, mysel = 0;
#pragma unroll
        for (int rd = 0; rd < 8; ++rd) {
            u32 m = wave_max_u32(c);
            c = (c == m) ? 0u : c;
            if (lane == rd) mysel = m;
            if (rd == 2 * w) k0 = m;
            if (rd == 2 * w + 1) k1 = m;
        }
        const int ta = 0xffff - (int)(k0 & 0xffffu), tbb = 0xffff - (int)(k1 & 0xffffu);
        const u32x4 ha0 = *(const u32x4*)(hbuf + (size_t)ta * D + 8 * lane);
        const u32 hb0 = *(const u32*)(hbuf + (size_t)ta * D + 512 + 2 * lane);
        const u32x4 ha1 = *(const u32x4*)(hbuf + (size_t)tbb * D + 8 * lane);
        const u32 hb1 = *(const u32*)(hbuf + (size_t)tbb * D + 512 + 2 * lane);
        const f32x2 st0 = *(const f32x2*)(stats + 2 * ta);
        const f32x2 st1 = *(const f32x2*)(stats + 2 * tbb);
#pragma unroll
        for (int i = 0; i < 5; ++i) {
            int ch = i * 256 + tid;
            *(u32x4*)(wg + ch * 8) = lg[i];
            *(u32x4*)(wu + ch * 8) = lu[i];
        }
        STAMP(3, 3);
#pragma unroll
        for (int j = 0; j < 2; ++j) {
            const int tok = 2 * w + j;
            const u32x4 ha = j ? ha1 : ha0;
            const u32 hb = j ? hb1 : hb0;
            const float m2 = j ? st1.x : st0.x, r2 = j ? st1.y : st0.y;
            float h[10];
            unpack10(ha, hb, h);
            u32 l2[5];
#pragma unroll
            for (int i = 0; i < 10; i += 2) {
                u32 dd = pk2(h[i] - m2, h[i + 1] - m2);
                l2[i >> 1] = pk2(lo2f(dd) * r2, hi2f(dd) * r2);
            }
            *(u32x4*)(xs + tok * XS + 8 * lane) = (u32x4){l2[0], l2[1], l2[2], l2[3]};
            *(u32*)(xs + tok * XS + 512 + 2 * lane) = l2[4];
            if (b == 0) {
                *(u32x4*)(selh + tok * D + 8 * lane) = ha;
                *(u32*)(selh + tok * D + 512 + 2 * lane) = hb;
                *(u32x4*)(sell2 + tok * D + 8 * lane) = (u32x4){l2[0], l2[1], l2[2], l2[3]};
                *(u32*)(sell2 + tok * D + 512 + 2 * lane) = l2[4];
            }
        }
        if (b == 0 && w == 0 && lane < 8) selkeys[lane] = mysel;
    }
    STAMP(3, 4);
    __syncthreads();
    STAMP(3, 5);
    {
        const int li = lane & 15, g = lane >> 4;
        f32x4 ag = {0.f, 0.f, 0.f, 0.f}, au = {0.f, 0.f, 0.f, 0.f};
        const int q4 = li >> 2, p4 = li & 3;
#pragma unroll
        for (int s = 0; s < 5; ++s) {
            const int k0 = (5 * w + s) * 32;
            bf16x8 af = *(const bf16x8*)(xs + (li & 7) * XS + k0 + 8 * g);
            const int ro = (k0 + 8 * g + q4) * 16 + 4 * p4;
            s16x4 g0 = tr_read(wg + ro), g1 = tr_read(wg + ro + 64);
            s16x4 u0 = tr_read(wu + ro), u1 = tr_read(wu + ro + 64);
            bf16x8 bg = __builtin_shufflevector(g0, g1, 0, 1, 2, 3, 4, 5, 6, 7);
            bf16x8 bu = __builtin_shufflevector(u0, u1, 0, 1, 2, 3, 4, 5, 6, 7);
            ag = __builtin_amdgcn_mfma_f32_16x16x32_bf16(af, bg, ag, 0, 0, 0);
            au = __builtin_amdgcn_mfma_f32_16x16x32_bf16(af, bu, au, 0, 0, 0);
        }
        if (g < 2) {
#pragma unroll
            for (int rr = 0; rr < 4; ++rr) {
                red[w][0][4 * g + rr][li] = ag[rr];
                red[w][1][4 * g + rr][li] = au[rr];
            }
        }
    }
    STAMP(3, 6);
    __syncthreads();
    if (tid < 128) {
        const int tok = tid >> 4, col = tid & 15;
        float gs = (red[0][0][tok][col] + red[1][0][tok][col]) + (red[2][0][tok][col] + red[3][0][tok][col]);
        float us = (red[0][1][tok][col] + red[1][1][tok][col]) + (red[2][1][tok][col] + red[3][1][tok][col]);
        float gb = rbf(gs), ub = rbf(us);
        float sg = rbf(gb / (1.0f + __expf(-gb)));
        u32 o = pk2(sg * ub, 0.f);
        gu[tok * F + f0 + col] = (u16)(o & 0xffffu);
    }
    STAMP(3, 7);
}

__global__ __launch_bounds__(256) void k4_down(const u16* __restrict__ gu, const u16* __restrict__ w_down,
                                               const u32* __restrict__ selkeys, const u16* __restrict__ selh,
                                               const u16* __restrict__ sell2, u16* __restrict__ out) {
    __shared__ __attribute__((aligned(16))) u16 wd[F * 8];
    __shared__ __attribute__((aligned(16))) float red[4][8][16];
    const int b = blockIdx.x;
    const int slice = (b & 7) * 10 + (b >> 3);
    const int d0 = slice * 8;
    const int tid = threadIdx.x, w = tid >> 6, lane = tid & 63;
    const int li = lane & 15, g = lane >> 4;
    STAMP(4, 0);
    u32x4 lw[7];
#pragma unroll
    for (int i = 0; i < 7; ++i) {
        int j = i * 2 + (lane >> 5);
        int s = w + 4 * j;
        if (s < 54) lw[i] = *(const u32x4*)(w_down + (size_t)(s * 32 + (lane & 31)) * D + d0);
    }
    bf16x8 af[14];
#pragma unroll
    for (int j = 0; j < 14; ++j) {
        int s = w + 4 * j;
        if (s < 54) af[j] = *(const bf16x8*)(gu + (li & 7) * F + s * 32 + 8 * g);
    }
    float hv = 0.f, l2v = 0.f;
    u32 key = 0;
    if (tid < 64) {
        const int tok = tid >> 3, col = tid & 7;
        key = selkeys[tok];
        hv = __uint_as_float(((u32)selh[tok * D + d0 + col]) << 16);
        l2v = __uint_as_float(((u32)sell2[tok * D + d0 + col]) << 16);
    }
    STAMP(4, 1);
#pragma unroll
    for (int i = 0; i < 7; ++i) {
        int j = i * 2 + (lane >> 5);
        int s = w + 4 * j;
        if (s < 54) *(u32x4*)(wd + (s * 32 + (lane & 31)) * 8) = lw[i];
    }
    __builtin_amdgcn_fence(__ATOMIC_RELEASE, "wavefront");
    __builtin_amdgcn_wave_barrier();
    __builtin_amdgcn_fence(__ATOMIC_ACQUIRE, "wavefront");
    STAMP(4, 2);
    {
        const int q4 = li >> 2, p4 = li & 1;
        f32x4 acc = {0.f, 0.f, 0.f, 0.f};
#pragma unroll
        for (int j = 0; j < 14; ++j) {
            int s = w + 4 * j;
            if (s < 54) {
                const int ro = (s * 32 + 8 * g + q4) * 8 + 4 * p4;
                s16x4 b0 = tr_read(wd + ro), b1 = tr_read(wd + ro + 32);
                bf16x8 bf = __builtin_shufflevector(b0, b1, 0, 1, 2, 3, 4, 5, 6, 7);
                acc = __builtin_amdgcn_mfma_f32_16x16x32_bf16(af[j], bf, acc, 0, 0, 0);
            }
        }
        if (g < 2) {
#pragma unroll
            for (int rr = 0; rr < 4; ++rr) red[w][4 * g + rr][li] = acc[rr];
        }
    }
    STAMP(4, 3);
    __syncthreads();
    if (tid < 64) {
        const int tok = tid >> 3, col = tid & 7;
        const int trow = 0xffff - (int)(key & 0xffffu);
        const float rv = key_r(key);
        const float rs = rbf(1.0f / (1.0f + __expf(-rv)));
        float s = (red[0][tok][col] + red[1][tok][col]) + (red[2][tok][col] + red[3][tok][col]);
        float y = rbf(s);
        y = rbf(y * rs);
        float o1 = rbf(l2v + y);
        u32 o = pk2(hv + o1, 0.f);
        out[(size_t)trow * D + d0 + col] = (u16)(o & 0xffffu);
    }
    STAMP(4, 4);
}

extern "C" void kernel_launch(void* const* d_in, const int* in_sizes, int n_in, void* d_out, int out_size,
                              void* d_ws, size_t ws_size, hipStream_t stream) {
    const u16* q = (const u16*)d_in[0];
    const u16* k = (const u16*)d_in[1];
    const u16* v = (const u16*)d_in[2];
    const u16* shift_k = (const u16*)d_in[3];
    const u16* ssm_k = (const u16*)d_in[4];
    const u16* ssm_D = (const u16*)d_in[5];
    const u16* w_r = (const u16*)d_in[7];
    const u16* w_gate = (const u16*)d_in[8];
    const u16* w_up = (const u16*)d_in[9];
    const u16* w_down = (const u16*)d_in[10];
    u16* out = (u16*)d_out;
    char* ws = (char*)d_ws;
    const size_t TS = (size_t)S * D * 2;
    u16* uT = (u16*)(ws);
    u16* y2T = (u16*)(ws + TS);
    u16* hbuf = (u16*)(ws + 2 * TS);
    char* sm = ws + 3 * TS;
    float* stats = (float*)(sm);
    u32* keys = (u32*)(sm + 16384);
    u32* selkeys = (u32*)(sm + 24576);
    u16* selh = (u16*)(sm + 24576 + 256);
    u16* sell2 = (u16*)(sm + 24576 + 256 + 10240);
    u16* gu = (u16*)(sm + 24576 + 256 + 20480);
#define PROF_MASK 0xff
    if (PROF_MASK & 1) k0_kv<<<512, 320, 0, stream>>>(k, v, shift_k, uT, ssm_k, q);
    if (PROF_MASK & 2) k1_conv<<<640, 256, 0, stream>>>(uT, ssm_k, ssm_D, y2T, w_gate, w_up, w_down);
    if (PROF_MASK & 4) k2_row<<<256, 512, 0, stream>>>(q, y2T, w_r, out, hbuf, stats, keys);
    if (PROF_MASK & 8) k3_gateup<<<108, 256, 0, stream>>>(keys, hbuf, stats, w_gate, w_up, gu, selkeys, selh, sell2);
    if (PROF_MASK & 16) k4_down<<<80, 256, 0, stream>>>(gu, w_down, selkeys, selh, sell2, out);
}
```

```cpp
#include <hip/hip_runtime.h>
#include <hip/hip_bf16.h>

typedef unsigned short u16;
typedef unsigned int u32;
typedef __attribute__((ext_vector_type(8))) short bf16x8;
typedef __attribute__((ext_vector_type(4))) short s16x4;
typedef __attribute__((ext_vector_type(4))) float f32x4;
typedef __attribute__((ext_vector_type(2))) float f32x2;
typedef __attribute__((ext_vector_type(2))) __bf16 bfx2;
typedef __attribute__((ext_vector_type(4))) unsigned u32x4;
typedef __attribute__((ext_vector_type(2))) unsigned u32x2;

constexpr int S = 2048, D = 640, F = 1728, NSEL = 8;

#define STAMP(kid, i)

__device__ __forceinline__ u32 pk2(float a, float b) {
    f32x2 v = {a, b};
    bfx2 r = __builtin_convertvector(v, bfx2);
    return __builtin_bit_cast(u32, r);
}
__device__ __forceinline__ float lo2f(u32 p) { return __uint_as_float(p << 16); }
__device__ __forceinline__ float hi2f(u32 p) { return __uint_as_float(p & 0xffff0000u); }
__device__ __forceinline__ float rbf(float a) { return lo2f(pk2(a, 0.f)); }

template <int CTRL, int RM> __device__ __forceinline__ int dpp_i(int v) {
    return __builtin_amdgcn_update_dpp(0, v, CTRL, RM, 0xf, true);
}
__device__ __forceinline__ float wave_sum(float v) {
    v += __int_as_float(dpp_i<0x111, 0xf>(__float_as_int(v)));
    v += __int_as_float(dpp_i<0x112, 0xf>(__float_as_int(v)));
    v += __int_as_float(dpp_i<0x114, 0xf>(__float_as_int(v)));
    v += __int_as_float(dpp_i<0x118, 0xf>(__float_as_int(v)));
    v += __int_as_float(dpp_i<0x142, 0xa>(__float_as_int(v)));
    v += __int_as_float(dpp_i<0x143, 0xc>(__float_as_int(v)));
    return __int_as_float(__builtin_amdgcn_readlane(__float_as_int(v), 63));
}
__device__ __forceinline__ u32 umax_(u32 a, u32 b) { return a > b ? a : b; }
__device__ __forceinline__ u32 wave_max_u32(u32 v) {
    v = umax_(v, (u32)dpp_i<0x111, 0xf>((int)v));
    v = umax_(v, (u32)dpp_i<0x112, 0xf>((int)v));
    v = umax_(v, (u32)dpp_i<0x114, 0xf>((int)v));
    v = umax_(v, (u32)dpp_i<0x118, 0xf>((int)v));
    v = umax_(v, (u32)dpp_i<0x142, 0xa>((int)v));
    v = umax_(v, (u32)dpp_i<0x143, 0xc>((int)v));
    return (u32)__builtin_amdgcn_readlane((int)v, 63);
}
#define KEEP4(val_) asm volatile("" ::"v"((val_).x), "v"((val_).y), "v"((val_).z), "v"((val_).w))

typedef __attribute__((address_space(3))) s16x4* lds_s16x4_ptr;
__device__ __forceinline__ s16x4 tr_read(const u16* p) {
    return __builtin_amdgcn_ds_read_tr16_b64_v4i16((lds_s16x4_ptr)p);
}
__device__ __forceinline__ void wave_lds_fence() {
    __builtin_amdgcn_fence(__ATOMIC_RELEASE, "wavefront");
    __builtin_amdgcn_wave_barrier();
    __builtin_amdgcn_fence(__ATOMIC_ACQUIRE, "wavefront");
}

__global__ __launch_bounds__(320) void k0_kv(const u16* __restrict__ k, const u16* __restrict__ v,
                                             const u16* __restrict__ shift_k, u16* __restrict__ uT,
                                             const u16* __restrict__ pf0, const u16* __restrict__ pf1) {
    const int b = blockIdx.x;
    const int rb = (b & 7) * 64 + (b >> 3);
    const int t0 = rb * 4;
    const int c = threadIdx.x;
    STAMP(0, 0);
    u32 kk[7], vv[4];
#pragma unroll
    for (int i = 0; i < 7; ++i) {
        int t = t0 - 3 + i;
        kk[i] = (t >= 0) ? *(const u32*)(k + (size_t)t * D + 2 * c) : 0u;
    }
#pragma unroll
    for (int i = 0; i < 4; ++i) vv[i] = *(const u32*)(v + (size_t)(t0 + i) * D + 2 * c);
    const u32x4 sw = *(const u32x4*)(shift_k + 8 * c);
    const u32x4 pa = *(const u32x4*)(pf0 + (size_t)(b * 320 + c) * 8);
    const u32x4 pb = *(const u32x4*)(pf1 + (size_t)(b * 320 + c) * 8);
    const float a0 = lo2f(sw.x), a1 = hi2f(sw.x), a2 = lo2f(sw.y), a3 = hi2f(sw.y);
    const float b0 = lo2f(sw.z), b1 = hi2f(sw.z), b2 = lo2f(sw.w), b3 = hi2f(sw.w);
    float ua[4], ub[4];
#pragma unroll
    for (int i = 0; i < 4; ++i) {
        u32 p0 = pk2(lo2f(kk[i]) * a3, hi2f(kk[i]) * b3);
        u32 p1 = pk2(lo2f(kk[i + 1]) * a2, hi2f(kk[i + 1]) * b2);
        u32 p2 = pk2(lo2f(kk[i + 2]) * a1, hi2f(kk[i + 2]) * b1);
        u32 p3 = pk2(lo2f(kk[i + 3]) * a0, hi2f(kk[i + 3]) * b0);
        u32 s = pk2(lo2f(p0) + lo2f(p1), hi2f(p0) + hi2f(p1));
        s = pk2(lo2f(s) + lo2f(p2), hi2f(s) + hi2f(p2));
        s = pk2(lo2f(s) + lo2f(p3), hi2f(s) + hi2f(p3));
        ua[i] = lo2f(s) * lo2f(vv[i]);
        ub[i] = hi2f(s) * hi2f(vv[i]);
    }
    u32x2 oa, ob;
    oa.x = pk2(ua[3], ua[2]); oa.y = pk2(ua[1], ua[0]);
    ob.x = pk2(ub[3], ub[2]); ob.y = pk2(ub[1], ub[0]);
    STAMP(0, 1);
    *(u32x2*)(uT + (size_t)(2 * c) * S + (t0 ^ 4)) = oa;
    *(u32x2*)(uT + (size_t)(2 * c + 1) * S + (t0 ^ 4)) = ob;
    KEEP4(pa);
    KEEP4(pb);
    STAMP(0, 2);
}

constexpr int UPAD = 256;
constexpr int KCS = 2080;
constexpr int WCH = 138240;

__global__ __launch_bounds__(256) void k1_conv(const u16* __restrict__ uT, const u16* __restrict__ ssm_k,
                                               const u16* __restrict__ ssm_D, u16* __restrict__ y2T,
                                               const u16* __restrict__ w_gate, const u16* __restrict__ w_up,
                                               const u16* __restrict__ w_down, u16* __restrict__ wgF,
                                               u16* __restrict__ wuF, u16* __restrict__ wdF) {
    __shared__ __attribute__((aligned(16))) u16 lds[UPAD + S + 16384];
    u16* uL = lds;
    u16* kL = lds + UPAD + S;
    f32x4* part = (f32x4*)(lds + UPAD + S);
    const int d = blockIdx.x;
    const int tid = threadIdx.x;
    const int w = tid >> 6, lane = tid & 63;
    if (blockIdx.x >= D) {
        u16* tl = lds + w * (2 * 32 * 72);
        const int T0 = ((blockIdx.x - D) * 4 + w) * 2;
        if (T0 >= 1620) return;
        const int li = lane & 15, g = lane >> 4;
        u32x4 ld[2][4];
#pragma unroll
        for (int tt = 0; tt < 2; ++tt) {
            const int T = T0 + tt;
            const int m = T / 540, rem = T - m * 540;
            const u16* src;
            int ldm;
            if (m < 2) { const int ks = rem / 27, cg = rem - ks * 27; src = (m ? w_up : w_gate) + (size_t)(ks * 32) * F + cg * 64; ldm = F; }
            else { const int ks = rem / 10, cg = rem - ks * 10; src = w_down + (size_t)(ks * 32) * D + cg * 64; ldm = D; }
#pragma unroll
            for (int i = 0; i < 4; ++i) {
                const int ch = i * 64 + lane;
                ld[tt][i] = *(const u32x4*)(src + (size_t)(ch >> 3) * ldm + (ch & 7) * 8);
            }
        }
#pragma unroll
        for (int tt = 0; tt < 2; ++tt)
#pragma unroll
            for (int i = 0; i < 4; ++i) {
                const int ch = i * 64 + lane;
                *(u32x4*)(tl + tt * (32 * 72) + (ch >> 3) * 72 + (ch & 7) * 8) = ld[tt][i];
            }
        wave_lds_fence();
#pragma unroll
        for (int tt = 0; tt < 2; ++tt) {
            const int T = T0 + tt;
            const int m = T / 540, rem = T - m * 540;
#pragma unroll
            for (int j = 0; j < 4; ++j) {
                const u16* rp = tl + tt * (32 * 72) + (8 * g + (li >> 2)) * 72 + 16 * j + 4 * (li & 3);
                s16x4 b0 = tr_read(rp), b1 = tr_read(rp + 4 * 72);
                bf16x8 fr = __builtin_shufflevector(b0, b1, 0, 1, 2, 3, 4, 5, 6, 7);
                u16* dst;
                if (m < 2) { const int ks = rem / 27, cg = rem - ks * 27; dst = (m ? wuF : wgF) + ((size_t)((cg * 4 + j) * 20 + ks) * 64 + lane) * 8; }
                else { const int ks = rem / 10, cg = rem - ks * 10; dst = wdF + ((size_t)((cg * 8 + 2 * j + (li >> 3)) * 54 + ks) * 32 + g * 8 + (li & 7)) * 8; }
                *(bf16x8*)dst = fr;
            }
        }
        return;
    }
    const u16* urow = uT + (size_t)d * S;
    const u16* krow = ssm_k + (size_t)d * S;
    STAMP(1, 0);
    const u32x4 ur = *(const u32x4*)(urow + tid * 8);
    const u32x4 kd = *(const u32x4*)(krow + tid * 8);
    u32x2 ke = {0u, 0u};
    if (tid < 255) ke = *(const u32x2*)(krow + tid * 8 + 8);
    const float Dv = __uint_as_float(((u32)ssm_D[d]) << 16);
    *(u32x4*)(uL + UPAD + tid * 8) = ur;
    const u32x4 z4 = {0u, 0u, 0u, 0u};
    if (tid < 32) *(u32x4*)(uL + tid * 8) = z4;
    {
        u32 d0 = kd.x, d1 = kd.y, d2 = kd.z, d3 = kd.w, d4 = ke.x, d5 = ke.y;
        u32 s0 = __builtin_amdgcn_alignbit(d1, d0, 16);
        u32 s1 = __builtin_amdgcn_alignbit(d2, d1, 16);
        u32 s2 = __builtin_amdgcn_alignbit(d3, d2, 16);
        u32 s3 = __builtin_amdgcn_alignbit(d4, d3, 16);
        u32 s4 = __builtin_amdgcn_alignbit(d5, d4, 16);
        u32x4 c0 = {d0, d1, d2, d3}, c1 = {s0, s1, s2, s3}, c2 = {d1, d2, d3, d4}, c3 = {s1, s2, s3, s4};
        const int Y = tid + 2;
        *(u32x4*)(kL + 0 * KCS + Y * 8) = c0;
        *(u32x4*)(kL + 1 * KCS + Y * 8) = c1;
        *(u32x4*)(kL + 2 * KCS + Y * 8) = c2;
        *(u32x4*)(kL + 3 * KCS + Y * 8) = c3;
        if (tid == 0) {
            u32x4 e1 = {0u, 0u, 0u, d0 << 16}, e2 = {0u, 0u, 0u, d0}, e3 = {0u, 0u, d0 << 16, s0};
            *(u32x4*)(kL + 0 * KCS + 0) = z4; *(u32x4*)(kL + 0 * KCS + 8) = z4;
            *(u32x4*)(kL + 1 * KCS + 0) = z4; *(u32x4*)(kL + 1 * KCS + 8) = e1;
            *(u32x4*)(kL + 2 * KCS + 0) = z4; *(u32x4*)(kL + 2 * KCS + 8) = e2;
            *(u32x4*)(kL + 3 * KCS + 0) = z4; *(u32x4*)(kL + 3 * KCS + 8) = e3;
        }
    }
    __syncthreads();
    STAMP(1, 1);
    const int li = lane & 15, g = lane >> 4;
    const u16* aBase = kL + ((li + 1) & 3) * KCS + ((li + 1) & ~3) + 8 * g + 64 * w;
    const u16* bBase = uL + UPAD + 16 * li + 8 - 8 * g - 64 * w;
    f32x4 acc[8];
#pragma unroll
    for (int c = 0; c < 8; ++c) acc[c] = (f32x4){0.f, 0.f, 0.f, 0.f};
    bf16x8 af[2][8], bf[2][8];
#pragma unroll
    for (int r = 0; r < 2; ++r) {
#pragma unroll
        for (int n = 0; n < 8; ++n) {
            const u16* p = aBase + 32 * (8 * n + r);
            s16x4 lo = *(const s16x4*)(p);
            s16x4 hi = *(const s16x4*)(p + 4);
            af[r][n] = __builtin_shufflevector(lo, hi, 0, 1, 2, 3, 4, 5, 6, 7);
            bf[r][n] = *(const bf16x8*)(bBase + 32 * (8 * n - r));
        }
    }
#pragma unroll
    for (int r = 0; r < 2; ++r)
#pragma unroll
        for (int n = 0; n < 8; ++n)
#pragma unroll
            for (int bq = 0; bq + n < 8; ++bq)
                acc[n + bq] = __builtin_amdgcn_mfma_f32_16x16x32_bf16(af[r][n], bf[r][bq], acc[n + bq], 0, 0, 0);
    STAMP(1, 2);
    __syncthreads();
#pragma unroll
    for (int c = 0; c < 8; ++c) part[(w * 8 + c) * 64 + lane] = acc[c];
    __syncthreads();
    STAMP(1, 3);
    u16* yrow = y2T + (size_t)d * S;
#pragma unroll
    for (int j = 0; j < 2; ++j) {
        const int c = 2 * w + j;
        f32x4 a0 = part[(0 * 8 + c) * 64 + lane], a1 = part[(1 * 8 + c) * 64 + lane];
        f32x4 a2 = part[(2 * 8 + c) * 64 + lane], a3 = part[(3 * 8 + c) * 64 + lane];
        f32x4 a = (a0 + a1) + (a2 + a3);
        const int tb = 256 * c + 16 * li + 8 * (g >> 1);
        u32x2 uu = *(const u32x2*)(uL + UPAD + tb + 4 * (1 - (g & 1)));
        float u3 = lo2f(uu.x), u2 = hi2f(uu.x), u1 = lo2f(uu.y), u0 = hi2f(uu.y);
        u32 y01 = pk2(a[0], a[1]);
        u32 y23 = pk2(a[2], a[3]);
        u32 w01 = pk2(u0 * Dv, u1 * Dv);
        u32 w23 = pk2(u2 * Dv, u3 * Dv);
        u32x2 o;
        o.x = pk2(lo2f(y01) + lo2f(w01), hi2f(y01) + hi2f(w01));
        o.y = pk2(lo2f(y23) + lo2f(w23), hi2f(y23) + hi2f(w23));
        *(u32x2*)(yrow + 256 * c + 16 * li + 4 * g) = o;
    }
    STAMP(1, 4);
}

__device__ __forceinline__ void unpack10(const u32x4& a, u32 b, float* f) {
    f[0] = lo2f(a.x); f[1] = hi2f(a.x); f[2] = lo2f(a.y); f[3] = hi2f(a.y);
    f[4] = lo2f(a.z); f[5] = hi2f(a.z); f[6] = lo2f(a.w); f[7] = hi2f(a.w);
    f[8] = lo2f(b); f[9] = hi2f(b);
}
__device__ __forceinline__ void ln_stats(const float* x, float& m_bf, float& s_bf) {
    float s = ((x[0] + x[1]) + (x[2] + x[3])) + ((x[4] + x[5]) + (x[6] + x[7])) + (x[8] + x[9]);
    s = wave_sum(s);
    const float mean = s * (1.0f / D);
    float q0 = 0.f, q1 = 0.f;
#pragma unroll
    for (int i = 0; i < 10; i += 2) {
        float d0 = x[i] - mean, d1 = x[i + 1] - mean;
        q0 += d0 * d0;
        q1 += d1 * d1;
    }
    float q = wave_sum(q0 + q1);
    const float var = q * (1.0f / D);
    m_bf = rbf(mean);
    float vb = rbf(var);
    float ve = rbf(vb + 1e-5f);
    s_bf = rbf(sqrtf(ve));
}

__global__ __launch_bounds__(512) void k2_row(const u16* __restrict__ q, const u16* __restrict__ y2T,
                                              const u16* __restrict__ w_r, u16* __restrict__ out,
                                              u16* __restrict__ hbuf, float* __restrict__ stats,
                                              u32* __restrict__ keys) {
    __shared__ __attribute__((aligned(16))) u32 yt[8][320];
    const int b = blockIdx.x;
    const int tb = (b & 7) * 32 + (b >> 3);
    const int t0 = tb * 8;
    const int tid = threadIdx.x;
    const int w = tid >> 6, lane = tid & 63;
    const int t = t0 + w;
    STAMP(2, 0);
    const u32x4 xa = *(const u32x4*)(q + (size_t)t * D + 8 * lane);
    const u32 xb = *(const u32*)(q + (size_t)t * D + 512 + 2 * lane);
    u32x4 ya = {0u, 0u, 0u, 0u}, yb = {0u, 0u, 0u, 0u};
    if (tid < 320) {
        ya = *(const u32x4*)(y2T + (size_t)(2 * tid) * S + t0);
        yb = *(const u32x4*)(y2T + (size_t)(2 * tid + 1) * S + t0);
    }
    const u32x4 wa = *(const u32x4*)(w_r + 8 * lane);
    const u32 wb = *(const u32*)(w_r + 512 + 2 * lane);
    float x[10];
    unpack10(xa, xb, x);
    float m1, s1;
    ln_stats(x, m1, s1);
    const float r1 = 1.0f / s1;
    STAMP(2, 1);
    if (tid < 320) {
        yt[0][tid] = __builtin_amdgcn_perm(yb.x, ya.x, 0x05040100);
        yt[1][tid] = __builtin_amdgcn_perm(yb.x, ya.x, 0x07060302);
        yt[2][tid] = __builtin_amdgcn_perm(yb.y, ya.y, 0x05040100);
        yt[3][tid] = __builtin_amdgcn_perm(yb.y, ya.y, 0x07060302);
        yt[4][tid] = __builtin_amdgcn_perm(yb.z, ya.z, 0x05040100);
        yt[5][tid] = __builtin_amdgcn_perm(yb.z, ya.z, 0x07060302);
        yt[6][tid] = __builtin_amdgcn_perm(yb.w, ya.w, 0x05040100);
        yt[7][tid] = __builtin_amdgcn_perm(yb.w, ya.w, 0x07060302);
    }
    u32 l1p[5];
#pragma unroll
    for (int i = 0; i < 10; i += 2) {
        u32 dd = pk2(x[i] - m1, x[i + 1] - m1);
        l1p[i >> 1] = pk2(lo2f(dd) * r1, hi2f(dd) * r1);
    }
    __syncthreads();
    STAMP(2, 2);
    const u32x4 y4 = *(const u32x4*)(&yt[w][4 * lane]);
    const u32 y1 = yt[w][256 + lane];
    float y[10];
    unpack10(y4, y1, y);
    float h[10];
    u32 hp[5], op[5];
#pragma unroll
    for (int i = 0; i < 10; i += 2) {
        u32 l1 = l1p[i >> 1];
        u32 a = pk2(lo2f(l1) * y[i], hi2f(l1) * y[i + 1]);
        u32 hh = pk2(x[i] + lo2f(a), x[i + 1] + hi2f(a));
        hp[i >> 1] = hh;
        h[i] = lo2f(hh);
        h[i + 1] = hi2f(hh);
    }
    *(u32x4*)(hbuf + (size_t)t * D + 8 * lane) = (u32x4){hp[0], hp[1], hp[2], hp[3]};
    *(u32*)(hbuf + (size_t)t * D + 512 + 2 * lane) = hp[4];
    float m2, s2;
    ln_stats(h, m2, s2);
    const float r2 = 1.0f / s2;
    float wr[10];
    unpack10(wa, wb, wr);
    float racc0 = 0.f, racc1 = 0.f;
#pragma unroll
    for (int i = 0; i < 10; i += 2) {
        u32 dd = pk2(h[i] - m2, h[i + 1] - m2);
        u32 l2 = pk2(lo2f(dd) * r2, hi2f(dd) * r2);
        racc0 += lo2f(l2) * wr[i];
        racc1 += hi2f(l2) * wr[i + 1];
        op[i >> 1] = pk2(h[i] + lo2f(l2), h[i + 1] + hi2f(l2));
    }
    *(u32x4*)(out + (size_t)t * D + 8 * lane) = (u32x4){op[0], op[1], op[2], op[3]};
    *(u32*)(out + (size_t)t * D + 512 + 2 * lane) = op[4];
    const float racc = wave_sum(racc0 + racc1);
    STAMP(2, 3);
    if (lane == 0) {
        u32 rb = pk2(racc, 0.f) & 0xffffu;
        u32 ord = (rb & 0x8000u) ? (rb ^ 0xffffu) : (rb ^ 0x8000u);
        keys[t] = (ord << 16) | (u32)(0xffff - t);
        *(f32x2*)(stats + 2 * t) = (f32x2){m2, r2};
    }
    STAMP(2, 4);
}

__device__ __forceinline__ float key_r(u32 key) {
    u32 ord = key >> 16;
    u32 rb = (ord & 0x8000u) ? (ord ^ 0x8000u) : (ord ^ 0xffffu);
    return __uint_as_float(rb << 16);
}

constexpr int XS = 648;
__global__ __launch_bounds__(256) void k3_gateup(const u32* __restrict__ keys, const u16* __restrict__ hbuf,
                                                 const float* __restrict__ stats, const u16* __restrict__ wgF,
                                                 const u16* __restrict__ wuF, u16* __restrict__ gu,
                                                 u32* __restrict__ selkeys, u16* __restrict__ selh,
                                                 u16* __restrict__ sell2) {
    __shared__ __attribute__((aligned(16))) u16 xs[8 * XS];
    __shared__ __attribute__((aligned(16))) float red[4][2][8][16];
    __shared__ u32 cand[32];
    const int b = blockIdx.x;
    const int slice = b;
    const int f0 = slice * 16;
    const int tid = threadIdx.x, w = tid >> 6, lane = tid & 63;
    STAMP(3, 0);
    u32 a[8];
    {
        u32x4 k0 = *(const u32x4*)(keys + tid * 8);
        u32x4 k1 = *(const u32x4*)(keys + tid * 8 + 4);
        a[0] = k0.x; a[1] = k0.y; a[2] = k0.z; a[3] = k0.w; a[4] = k1.x; a[5] = k1.y; a[6] = k1.z; a[7] = k1.w;
    }
    bf16x8 bg[5], bu[5];
#pragma unroll
    for (int i = 0; i < 5; ++i) {
        bg[i] = *(const bf16x8*)(wgF + ((size_t)(slice * 20 + 5 * w + i) * 64 + lane) * 8);
        bu[i] = *(const bf16x8*)(wuF + ((size_t)(slice * 20 + 5 * w + i) * 64 + lane) * 8);
    }
#define CE(i, j) { u32 hi_ = a[i] > a[j] ? a[i] : a[j]; u32 lo_ = a[i] > a[j] ? a[j] : a[i]; a[i] = hi_; a[j] = lo_; }
    CE(0, 1) CE(2, 3) CE(4, 5) CE(6, 7)
    CE(0, 2) CE(1, 3) CE(4, 6) CE(5, 7)
    CE(1, 2) CE(5, 6) CE(0, 4) CE(3, 7)
    CE(1, 5) CE(2, 6)
    CE(1, 4) CE(3, 6)
    CE(2, 4) CE(3, 5)
    CE(3, 4)
#undef CE
    u32 mine = 0;
#pragma unroll
    for (int rd = 0; rd < 8; ++rd) {
        u32 m = wave_max_u32(a[0]);
        bool win = (a[0] == m);
#pragma unroll
        for (int i = 0; i < 7 - rd; ++i) a[i] = win ? a[i + 1] : a[i];
        if (lane == rd) mine = m;
    }
    if (lane < 8) cand[w * 8 + lane] = mine;
    STAMP(3, 1);
    __syncthreads();
    STAMP(3, 2);
    {
        u32 c = (lane < 32) ? cand[lane] : 0u;
        u32 k0 = 0, k1 = 0, mysel = 0;
#pragma unroll
        for (int rd = 0; rd < 8; ++rd) {
            u32 m = wave_max_u32(c);
            c = (c == m) ? 0u : c;
            if (lane == rd) mysel = m;
            if (rd == 2 * w) k0 = m;
            if (rd == 2 * w + 1) k1 = m;
        }
        const int ta = 0xffff - (int)(k0 & 0xffffu), tbb = 0xffff - (int)(k1 & 0xffffu);
        const u32x4 ha0 = *(const u32x4*)(hbuf + (size_t)ta * D + 8 * lane);
        const u32 hb0 = *(const u32*)(hbuf + (size_t)ta * D + 512 + 2 * lane);
        const u32x4 ha1 = *(const u32x4*)(hbuf + (size_t)tbb * D + 8 * lane);
        const u32 hb1 = *(const u32*)(hbuf + (size_t)tbb * D + 512 + 2 * lane);
        const f32x2 st0 = *(const f32x2*)(stats + 2 * ta);
        const f32x2 st1 = *(const f32x2*)(stats + 2 * tbb);
        STAMP(3, 3);
#pragma unroll
        for (int j = 0; j < 2; ++j) {
            const int tok = 2 * w + j;
            const u32x4 ha = j ? ha1 : ha0;
            const u32 hb = j ? hb1 : hb0;
            const float m2 = j ? st1.x : st0.x, r2 = j ? st1.y : st0.y;
            float h[10];
            unpack10(ha, hb, h);
            u32 l2[5];
#pragma unroll
            for (int i = 0; i < 10; i += 2) {
                u32 dd = pk2(h[i] - m2, h[i + 1] - m2);
                l2[i >> 1] = pk2(lo2f(dd) * r2, hi2f(dd) * r2);
            }
            *(u32x4*)(xs + tok * XS + 8 * lane) = (u32x4){l2[0], l2[1], l2[2], l2[3]};
            *(u32*)(xs + tok * XS + 512 + 2 * lane) = l2[4];
            if (b == 0) {
                *(u32x4*)(selh + tok * D + 8 * lane) = ha;
                *(u32*)(selh + tok * D + 512 + 2 * lane) = hb;
                *(u32x4*)(sell2 + tok * D + 8 * lane) = (u32x4){l2[0], l2[1], l2[2], l2[3]};
                *(u32*)(sell2 + tok * D + 512 + 2 * lane) = l2[4];
            }
        }
        if (b == 0 && w == 0 && lane < 8) selkeys[lane] = mysel;
    }
    STAMP(3, 4);
    __syncthreads();
    STAMP(3, 5);
    {
        const int li = lane & 15, g = lane >> 4;
        f32x4 ag = {0.f, 0.f, 0.f, 0.f}, au = {0.f, 0.f, 0.f, 0.f};
#pragma unroll
        for (int s = 0; s < 5; ++s) {
            const int k0 = (5 * w + s) * 32;
            bf16x8 af = *(const bf16x8*)(xs + (li & 7) * XS + k0 + 8 * g);
            ag = __builtin_amdgcn_mfma_f32_16x16x32_bf16(af, bg[s], ag, 0, 0, 0);
            au = __builtin_amdgcn_mfma_f32_16x16x32_bf16(af, bu[s], au, 0, 0, 0);
        }
        if (g < 2) {
#pragma unroll
            for (int rr = 0; rr < 4; ++rr) {
                red[w][0][4 * g + rr][li] = ag[rr];
                red[w][1][4 * g + rr][li] = au[rr];
            }
        }
    }
    STAMP(3, 6);
    __syncthreads();
    if (tid < 128) {
        const int tok = tid >> 4, col = tid & 15;
        float gs = (red[0][0][tok][col] + red[1][0][tok][col]) + (red[2][0][tok][col] + red[3][0][tok][col]);
        float us = (red[0][1][tok][col] + red[1][1][tok][col]) + (red[2][1][tok][col] + red[3][1][tok][col]);
        float gb = rbf(gs), ub = rbf(us);
        float sg = rbf(gb / (1.0f + __expf(-gb)));
        u32 o = pk2(sg * ub, 0.f);
        gu[tok * F + f0 + col] = (u16)(o & 0xffffu);
    }
    STAMP(3, 7);
}

__global__ __launch_bounds__(512) void k4_down(const u16* __restrict__ gu, const u16* __restrict__ wdF,
                                               const u32* __restrict__ selkeys, const u16* __restrict__ selh,
                                               const u16* __restrict__ sell2, u16* __restrict__ out) {
    __shared__ __attribute__((aligned(16))) float red[8][8][16];
    const int slice = blockIdx.x;
    const int d0 = slice * 8;
    const int tid = threadIdx.x, w = tid >> 6, lane = tid & 63;
    const int li = lane & 15, g = lane >> 4;
    STAMP(4, 0);
    bf16x8 af[7], bf[7];
#pragma unroll
    for (int j = 0; j < 7; ++j) {
        int s = w + 8 * j;
        if (s < 54) {
            af[j] = *(const bf16x8*)(gu + (li & 7) * F + s * 32 + 8 * g);
            bf[j] = *(const bf16x8*)(wdF + ((size_t)(slice * 54 + s) * 32 + g * 8 + (li & 7)) * 8);
        }
    }
    float hv = 0.f, l2v = 0.f;
    u32 key = 0;
    if (tid < 64) {
        const int tok = tid >> 3, col = tid & 7;
        key = selkeys[tok];
        hv = __uint_as_float(((u32)selh[tok * D + d0 + col]) << 16);
        l2v = __uint_as_float(((u32)sell2[tok * D + d0 + col]) << 16);
    }
    STAMP(4, 1);
    {
        f32x4 acc = {0.f, 0.f, 0.f, 0.f};
#pragma unroll
        for (int j = 0; j < 7; ++j) {
            int s = w + 8 * j;
            if (s < 54) acc = __builtin_amdgcn_mfma_f32_16x16x32_bf16(af[j], bf[j], acc, 0, 0, 0);
        }
        if (g < 2) {
#pragma unroll
            for (int rr = 0; rr < 4; ++rr) red[w][4 * g + rr][li] = acc[rr];
        }
    }
    STAMP(4, 2);
    __syncthreads();
    if (tid < 64) {
        const int tok = tid >> 3, col = tid & 7;
        const int trow = 0xffff - (int)(key & 0xffffu);
        const float rv = key_r(key);
        const float rs = rbf(1.0f / (1.0f + __expf(-rv)));
        float s = ((red[0][tok][col] + red[1][tok][col]) + (red[2][tok][col] + red[3][tok][col])) +
                  ((red[4][tok][col] + red[5][tok][col]) + (red[6][tok][col] + red[7][tok][col]));
        float y = rbf(s);
        y = rbf(y * rs);
        float o1 = rbf(l2v + y);
        u32 o = pk2(hv + o1, 0.f);
        out[(size_t)trow * D + d0 + col] = (u16)(o & 0xffffu);
    }
    STAMP(4, 3);
}

extern "C" void kernel_launch(void* const* d_in, const int* in_sizes, int n_in, void* d_out, int out_size,
                              void* d_ws, size_t ws_size, hipStream_t stream) {
    const u16* q = (const u16*)d_in[0];
    const u16* k = (const u16*)d_in[1];
    const u16* v = (const u16*)d_in[2];
    const u16* shift_k = (const u16*)d_in[3];
    const u16* ssm_k = (const u16*)d_in[4];
    const u16* ssm_D = (const u16*)d_in[5];
    const u16* w_r = (const u16*)d_in[7];
    const u16* w_gate = (const u16*)d_in[8];
    const u16* w_up = (const u16*)d_in[9];
    const u16* w_down = (const u16*)d_in[10];
    u16* out = (u16*)d_out;
    char* ws = (char*)d_ws;
    const size_t TS = (size_t)S * D * 2;
    u16* uT = (u16*)(ws);
    u16* y2T = (u16*)(ws + TS);
    u16* hbuf = (u16*)(ws + 2 * TS);
    char* sm = ws + 3 * TS;
    float* stats = (float*)(sm);
    u32* keys = (u32*)(sm + 16384);
    u32* selkeys = (u32*)(sm + 24576);
    u16* selh = (u16*)(sm + 24576 + 256);
    u16* sell2 = (u16*)(sm + 24576 + 256 + 10240);
    u16* gu = (u16*)(sm + 24576 + 256 + 20480);
    const size_t WS_ = (size_t)D * F * 2;
    u16* wgF = (u16*)(sm + 131072);
    u16* wuF = (u16*)(sm + 131072 + WS_);
    u16* wdF = (u16*)(sm + 131072 + 2 * WS_);
#define PROF_MASK 0xff
    if (PROF_MASK & 1) k0_kv<<<512, 320, 0, stream>>>(k, v, shift_k, uT, ssm_k, q);
    if (PROF_MASK & 2) k1_conv<<<640 + 203, 256, 0, stream>>>(uT, ssm_k, ssm_D, y2T, w_gate, w_up, w_down, wgF, wuF, wdF);
    if (PROF_MASK & 4) k2_row<<<256, 512, 0, stream>>>(q, y2T, w_r, out, hbuf, stats, keys);
    if (PROF_MASK & 8) k3_gateup<<<108, 256, 0, stream>>>(keys, hbuf, stats, wgF, wuF, gu, selkeys, selh, sell2);
    if (PROF_MASK & 16) k4_down<<<80, 512, 0, stream>>>(gu, wdF, selkeys, selh, sell2, out);
}
```

```cpp
#include <hip/hip_runtime.h>
#include <hip/hip_bf16.h>

typedef unsigned short u16;
typedef unsigned int u32;
typedef __attribute__((ext_vector_type(8))) short bf16x8;
typedef __attribute__((ext_vector_type(4))) short s16x4;
typedef __attribute__((ext_vector_type(4))) float f32x4;
typedef __attribute__((ext_vector_type(2))) float f32x2;
typedef __attribute__((ext_vector_type(2))) __bf16 bfx2;
typedef __attribute__((ext_vector_type(4))) unsigned u32x4;
typedef __attribute__((ext_vector_type(2))) unsigned u32x2;

constexpr int S = 2048, D = 640, F = 1728, NSEL = 8;

#define STAMP(kid, i)

__device__ __forceinline__ u32 pk2(float a, float b) {
    f32x2 v = {a, b};
    bfx2 r = __builtin_convertvector(v, bfx2);
    return __builtin_bit_cast(u32, r);
}
__device__ __forceinline__ float lo2f(u32 p) { return __uint_as_float(p << 16); }
__device__ __forceinline__ float hi2f(u32 p) { return __uint_as_float(p & 0xffff0000u); }
__device__ __forceinline__ float rbf(float a) { return lo2f(pk2(a, 0.f)); }

template <int CTRL, int RM> __device__ __forceinline__ int dpp_i(int v) {
    return __builtin_amdgcn_update_dpp(0, v, CTRL, RM, 0xf, true);
}
__device__ __forceinline__ float wave_sum(float v) {
    v += __int_as_float(dpp_i<0x111, 0xf>(__float_as_int(v)));
    v += __int_as_float(dpp_i<0x112, 0xf>(__float_as_int(v)));
    v += __int_as_float(dpp_i<0x114, 0xf>(__float_as_int(v)));
    v += __int_as_float(dpp_i<0x118, 0xf>(__float_as_int(v)));
    v += __int_as_float(dpp_i<0x142, 0xa>(__float_as_int(v)));
    v += __int_as_float(dpp_i<0x143, 0xc>(__float_as_int(v)));
    return __int_as_float(__builtin_amdgcn_readlane(__float_as_int(v), 63));
}
__device__ __forceinline__ u32 umax_(u32 a, u32 b) { return a > b ? a : b; }
__device__ __forceinline__ u32 wave_max_u32(u32 v) {
    v = umax_(v, (u32)dpp_i<0x111, 0xf>((int)v));
    v = umax_(v, (u32)dpp_i<0x112, 0xf>((int)v));
    v = umax_(v, (u32)dpp_i<0x114, 0xf>((int)v));
    v = umax_(v, (u32)dpp_i<0x118, 0xf>((int)v));
    v = umax_(v, (u32)dpp_i<0x142, 0xa>((int)v));
    v = umax_(v, (u32)dpp_i<0x143, 0xc>((int)v));
    return (u32)__builtin_amdgcn_readlane((int)v, 63);
}
#define KEEP4(val_) asm volatile("" ::"v"((val_).x), "v"((val_).y), "v"((val_).z), "v"((val_).w))

typedef __attribute__((address_space(3))) s16x4* lds_s16x4_ptr;
__device__ __forceinline__ s16x4 tr_read(const u16* p) {
    return __builtin_amdgcn_ds_read_tr16_b64_v4i16((lds_s16x4_ptr)p);
}
__device__ __forceinline__ void wave_lds_fence() {
    __builtin_amdgcn_fence(__ATOMIC_RELEASE, "wavefront");
    __builtin_amdgcn_wave_barrier();
    __builtin_amdgcn_fence(__ATOMIC_ACQUIRE, "wavefront");
}

__global__ __launch_bounds__(512) void k0_kv(const u16* __restrict__ k, const u16* __restrict__ v,
                                             const u16* __restrict__ shift_k, u16* __restrict__ uT,
                                             const u16* __restrict__ pf0, const u16* __restrict__ pf1) {
    __shared__ __attribute__((aligned(16))) u16 tile[64 * 72];
    const int b = blockIdx.x;
    const int ct = b % 10, rt = b / 10;
    const int tid = threadIdx.x;
    const int cp = tid & 31, rg = tid >> 5;
    const int t0 = rt * 64 + rg * 4;
    const int c = ct * 32 + cp;
    STAMP(0, 0);
    u32 kk[7], vv[4];
#pragma unroll
    for (int i = 0; i < 7; ++i) {
        int t = t0 - 3 + i;
        kk[i] = (t >= 0) ? *(const u32*)(k + (size_t)t * D + 2 * c) : 0u;
    }
#pragma unroll
    for (int i = 0; i < 4; ++i) vv[i] = *(const u32*)(v + (size_t)(t0 + i) * D + 2 * c);
    const u32x4 sw = *(const u32x4*)(shift_k + 8 * c);
    const u32x4 pa = *(const u32x4*)(pf0 + (size_t)(b * 512 + tid) * 8);
    const u32x4 pb = *(const u32x4*)(pf1 + (size_t)(b * 512 + tid) * 8);
    const float a0 = lo2f(sw.x), a1 = hi2f(sw.x), a2 = lo2f(sw.y), a3 = hi2f(sw.y);
    const float b0 = lo2f(sw.z), b1 = hi2f(sw.z), b2 = lo2f(sw.w), b3 = hi2f(sw.w);
    float ua[4], ub[4];
#pragma unroll
    for (int i = 0; i < 4; ++i) {
        u32 p0 = pk2(lo2f(kk[i]) * a3, hi2f(kk[i]) * b3);
        u32 p1 = pk2(lo2f(kk[i + 1]) * a2, hi2f(kk[i + 1]) * b2);
        u32 p2 = pk2(lo2f(kk[i + 2]) * a1, hi2f(kk[i + 2]) * b1);
        u32 p3 = pk2(lo2f(kk[i + 3]) * a0, hi2f(kk[i + 3]) * b0);
        u32 s = pk2(lo2f(p0) + lo2f(p1), hi2f(p0) + hi2f(p1));
        s = pk2(lo2f(s) + lo2f(p2), hi2f(s) + hi2f(p2));
        s = pk2(lo2f(s) + lo2f(p3), hi2f(s) + hi2f(p3));
        ua[i] = lo2f(s) * lo2f(vv[i]);
        ub[i] = hi2f(s) * hi2f(vv[i]);
    }
    u32x2 oa, ob;
    oa.x = pk2(ua[3], ua[2]); oa.y = pk2(ua[1], ua[0]);
    ob.x = pk2(ub[3], ub[2]); ob.y = pk2(ub[1], ub[0]);
    *(u32x2*)(tile + (2 * cp) * 72 + ((rg * 4) ^ 4)) = oa;
    *(u32x2*)(tile + (2 * cp + 1) * 72 + ((rg * 4) ^ 4)) = ob;
    STAMP(0, 1);
    __syncthreads();
    {
        const int ch = tid >> 3, pc = tid & 7;
        const u32x4 o = *(const u32x4*)(tile + ch * 72 + pc * 8);
        *(u32x4*)(uT + (size_t)(ct * 64 + ch) * S + rt * 64 + pc * 8) = o;
    }
    KEEP4(pa);
    KEEP4(pb);
    STAMP(0, 2);
}

constexpr int UPAD = 256;
constexpr int KCS = 2080;
constexpr int WCH = 138240;

template <int HALF>
__device__ __forceinline__ void conv_body(const u16* uL, const u16* kL, f32x4* part, int w, int lane, float Dv,
                                          u16* __restrict__ yrow) {
    constexpr int NT = HALF ? 5 : 3;
    constexpr int NF = HALF ? 6 : 8;
    constexpr int tiles[5] = {HALF ? 5 : 7, HALF ? 4 : 6, HALF ? 3 : 2, 1, 0};
    const int li = lane & 15, g = lane >> 4;
    const u16* aBase = kL + ((li + 1) & 3) * KCS + ((li + 1) & ~3) + 8 * g + 64 * w;
    const u16* bBase = uL + UPAD + 16 * li + 8 - 8 * g - 64 * w;
    f32x4 acc[NT];
#pragma unroll
    for (int c = 0; c < NT; ++c) acc[c] = (f32x4){0.f, 0.f, 0.f, 0.f};
    bf16x8 af[2][NF], bf[2][NF];
#pragma unroll
    for (int r = 0; r < 2; ++r) {
#pragma unroll
        for (int n = 0; n < NF; ++n) {
            const u16* p = aBase + 32 * (8 * n + r);
            s16x4 lo = *(const s16x4*)(p);
            s16x4 hi = *(const s16x4*)(p + 4);
            af[r][n] = __builtin_shufflevector(lo, hi, 0, 1, 2, 3, 4, 5, 6, 7);
            bf[r][n] = *(const bf16x8*)(bBase + 32 * (8 * n - r));
        }
    }
#pragma unroll
    for (int r = 0; r < 2; ++r)
#pragma unroll
        for (int n = 0; n < NF; ++n)
#pragma unroll
            for (int c = 0; c < NT; ++c)
                if (tiles[c] - n >= 0)
                    acc[c] = __builtin_amdgcn_mfma_f32_16x16x32_bf16(af[r][n], bf[r][tiles[c] - n], acc[c], 0, 0, 0);
    STAMP(1, 2);
    __syncthreads();
#pragma unroll
    for (int c = 0; c < NT; ++c) part[(w * NT + c) * 64 + lane] = acc[c];
    __syncthreads();
    STAMP(1, 3);
#pragma unroll
    for (int it = 0; it < (NT + 3) / 4; ++it) {
        const int ci = it * 4 + w;
        if (ci < NT) {
            int c = tiles[0];
#pragma unroll
            for (int q = 1; q < NT; ++q) c = (ci == q) ? tiles[q] : c;
            f32x4 a0 = part[(0 * NT + ci) * 64 + lane], a1 = part[(1 * NT + ci) * 64 + lane];
            f32x4 a2 = part[(2 * NT + ci) * 64 + lane], a3 = part[(3 * NT + ci) * 64 + lane];
            f32x4 a = (a0 + a1) + (a2 + a3);
            const int tb = 256 * c + 16 * li + 8 * (g >> 1);
            u32x2 uu = *(const u32x2*)(uL + UPAD + tb + 4 * (1 - (g & 1)));
            float u3 = lo2f(uu.x), u2 = hi2f(uu.x), u1 = lo2f(uu.y), u0 = hi2f(uu.y);
            u32 y01 = pk2(a[0], a[1]);
            u32 y23 = pk2(a[2], a[3]);
            u32 w01 = pk2(u0 * Dv, u1 * Dv);
            u32 w23 = pk2(u2 * Dv, u3 * Dv);
            u32x2 o;
            o.x = pk2(lo2f(y01) + lo2f(w01), hi2f(y01) + hi2f(w01));
            o.y = pk2(lo2f(y23) + lo2f(w23), hi2f(y23) + hi2f(w23));
            *(u32x2*)(yrow + 256 * c + 16 * li + 4 * g) = o;
        }
    }
}

constexpr int K1_CONV_WGS = 2 * D;
__global__ __launch_bounds__(256) void k1_conv(const u16* __restrict__ uT, const u16* __restrict__ ssm_k,
                                               const u16* __restrict__ ssm_D, u16* __restrict__ y2T,
                                               const u16* __restrict__ w_gate, const u16* __restrict__ w_up,
                                               const u16* __restrict__ w_down, u16* __restrict__ wgF,
                                               u16* __restrict__ wuF, u16* __restrict__ wdF) {
    __shared__ __attribute__((aligned(16))) u16 lds[UPAD + S + 10240];
    u16* uL = lds;
    u16* kL = lds + UPAD + S;
    f32x4* part = (f32x4*)(lds + UPAD + S);
    const int tid = threadIdx.x;
    const int w = tid >> 6, lane = tid & 63;
    if (blockIdx.x >= K1_CONV_WGS) {
        u16* tl = lds + w * (32 * 72);
        const int T = (blockIdx.x - K1_CONV_WGS) * 4 + w;
        const int li = lane & 15, g = lane >> 4;
        const int m = T / 540, rem = T - m * 540;
        const u16* src;
        int ldm, ks, cg;
        if (m < 2) { ks = rem / 27; cg = rem - ks * 27; src = (m ? w_up : w_gate) + (size_t)(ks * 32) * F + cg * 64; ldm = F; }
        else { ks = rem / 10; cg = rem - ks * 10; src = w_down + (size_t)(ks * 32) * D + cg * 64; ldm = D; }
        u32x4 ld[4];
#pragma unroll
        for (int i = 0; i < 4; ++i) {
            const int ch = i * 64 + lane;
            ld[i] = *(const u32x4*)(src + (size_t)(ch >> 3) * ldm + (ch & 7) * 8);
        }
#pragma unroll
        for (int i = 0; i < 4; ++i) {
            const int ch = i * 64 + lane;
            *(u32x4*)(tl + (ch >> 3) * 72 + (ch & 7) * 8) = ld[i];
        }
        wave_lds_fence();
#pragma unroll
        for (int j = 0; j < 4; ++j) {
            const u16* rp = tl + (8 * g + (li >> 2)) * 72 + 16 * j + 4 * (li & 3);
            s16x4 b0 = tr_read(rp), b1 = tr_read(rp + 4 * 72);
            bf16x8 fr = __builtin_shufflevector(b0, b1, 0, 1, 2, 3, 4, 5, 6, 7);
            u16* dst;
            if (m < 2) dst = (m ? wuF : wgF) + ((size_t)((cg * 4 + j) * 20 + ks) * 64 + lane) * 8;
            else dst = wdF + ((size_t)((cg * 8 + 2 * j + (li >> 3)) * 54 + ks) * 32 + g * 8 + (li & 7)) * 8;
            asm volatile("global_store_dwordx4 %0, %1, off sc1\n\ts_nop 1" ::"v"(dst), "v"(fr) : "memory");
        }
        return;
    }
    const int d = blockIdx.x >> 1;
    const int half = blockIdx.x & 1;
    const u16* urow = uT + (size_t)d * S;
    const u16* krow = ssm_k + (size_t)d * S;
    STAMP(1, 0);
    const u32x4 z4 = {0u, 0u, 0u, 0u};
    u32x4 ur = z4, kd = z4;
    u32x2 ke = {0u, 0u};
    if (half == 0 || tid < 192) {
        ur = *(const u32x4*)(urow + tid * 8);
        kd = *(const u32x4*)(krow + tid * 8);
        if (tid < 255) ke = *(const u32x2*)(krow + tid * 8 + 8);
    }
    const float Dv = __uint_as_float(((u32)ssm_D[d]) << 16);
    *(u32x4*)(uL + UPAD + tid * 8) = ur;
    if (tid < 32) *(u32x4*)(uL + tid * 8) = z4;
    {
        u32 d0 = kd.x, d1 = kd.y, d2 = kd.z, d3 = kd.w, d4 = ke.x, d5 = ke.y;
        u32 s0 = __builtin_amdgcn_alignbit(d1, d0, 16);
        u32 s1 = __builtin_amdgcn_alignbit(d2, d1, 16);
        u32 s2 = __builtin_amdgcn_alignbit(d3, d2, 16);
        u32 s3 = __builtin_amdgcn_alignbit(d4, d3, 16);
        u32 s4 = __builtin_amdgcn_alignbit(d5, d4, 16);
        u32x4 c0 = {d0, d1, d2, d3}, c1 = {s0, s1, s2, s3}, c2 = {d1, d2, d3, d4}, c3 = {s1, s2, s3, s4};
        const int Y = tid + 2;
        *(u32x4*)(kL + 0 * KCS + Y * 8) = c0;
        *(u32x4*)(kL + 1 * KCS + Y * 8) = c1;
        *(u32x4*)(kL + 2 * KCS + Y * 8) = c2;
        *(u32x4*)(kL + 3 * KCS + Y * 8) = c3;
        if (tid == 0) {
            u32x4 e1 = {0u, 0u, 0u, d0 << 16}, e2 = {0u, 0u, 0u, d0}, e3 = {0u, 0u, d0 << 16, s0};
            *(u32x4*)(kL + 0 * KCS + 0) = z4; *(u32x4*)(kL + 0 * KCS + 8) = z4;
            *(u32x4*)(kL + 1 * KCS + 0) = z4; *(u32x4*)(kL + 1 * KCS + 8) = e1;
            *(u32x4*)(kL + 2 * KCS + 0) = z4; *(u32x4*)(kL + 2 * KCS + 8) = e2;
            *(u32x4*)(kL + 3 * KCS + 0) = z4; *(u32x4*)(kL + 3 * KCS + 8) = e3;
        }
    }
    __syncthreads();
    STAMP(1, 1);
    u16* yrow = y2T + (size_t)d * S;
    if (half == 0) conv_body<0>(uL, kL, part, w, lane, Dv, yrow);
    else conv_body<1>(uL, kL, part, w, lane, Dv, yrow);
    STAMP(1, 4);
}

__device__ __forceinline__ void unpack10(const u32x4& a, u32 b, float* f) {
    f[0] = lo2f(a.x); f[1] = hi2f(a.x); f[2] = lo2f(a.y); f[3] = hi2f(a.y);
    f[4] = lo2f(a.z); f[5] = hi2f(a.z); f[6] = lo2f(a.w); f[7] = hi2f(a.w);
    f[8] = lo2f(b); f[9] = hi2f(b);
}
__device__ __forceinline__ void ln_stats(const float* x, float& m_bf, float& s_bf) {
    float s = ((x[0] + x[1]) + (x[2] + x[3])) + ((x[4] + x[5]) + (x[6] + x[7])) + (x[8] + x[9]);
    s = wave_sum(s);
    const float mean = s * (1.0f / D);
    float q0 = 0.f, q1 = 0.f;
#pragma unroll
    for (int i = 0; i < 10; i += 2) {
        float d0 = x[i] - mean, d1 = x[i + 1] - mean;
        q0 += d0 * d0;
        q1 += d1 * d1;
    }
    float q = wave_sum(q0 + q1);
    const float var = q * (1.0f / D);
    m_bf = rbf(mean);
    float vb = rbf(var);
    float ve = rbf(vb + 1e-5f);
    s_bf = rbf(sqrtf(ve));
}

__global__ __launch_bounds__(512) void k2_row(const u16* __restrict__ q, const u16* __restrict__ y2T,
                                              const u16* __restrict__ w_r, u16* __restrict__ out,
                                              u16* __restrict__ hbuf, float* __restrict__ stats,
                                              u32* __restrict__ keys) {
    __shared__ __attribute__((aligned(16))) u32 yt[8][320];
    const int b = blockIdx.x;
    const int tb = (b & 7) * 32 + (b >> 3);
    const int t0 = tb * 8;
    const int tid = threadIdx.x;
    const int w = tid >> 6, lane = tid & 63;
    const int t = t0 + w;
    STAMP(2, 0);
    const u32x4 xa = *(const u32x4*)(q + (size_t)t * D + 8 * lane);
    const u32 xb = *(const u32*)(q + (size_t)t * D + 512 + 2 * lane);
    u32x4 ya = {0u, 0u, 0u, 0u}, yb = {0u, 0u, 0u, 0u};
    if (tid < 320) {
        ya = *(const u32x4*)(y2T + (size_t)(2 * tid) * S + t0);
        yb = *(const u32x4*)(y2T + (size_t)(2 * tid + 1) * S + t0);
    }
    const u32x4 wa = *(const u32x4*)(w_r + 8 * lane);
    const u32 wb = *(const u32*)(w_r + 512 + 2 * lane);
    float x[10];
    unpack10(xa, xb, x);
    float m1, s1;
    ln_stats(x, m1, s1);
    const float r1 = 1.0f / s1;
    STAMP(2, 1);
    if (tid < 320) {
        yt[0][tid] = __builtin_amdgcn_perm(yb.x, ya.x, 0x05040100);
        yt[1][tid] = __builtin_amdgcn_perm(yb.x, ya.x, 0x07060302);
        yt[2][tid] = __builtin_amdgcn_perm(yb.y, ya.y, 0x05040100);
        yt[3][tid] = __builtin_amdgcn_perm(yb.y, ya.y, 0x07060302);
        yt[4][tid] = __builtin_amdgcn_perm(yb.z, ya.z, 0x05040100);
        yt[5][tid] = __builtin_amdgcn_perm(yb.z, ya.z, 0x07060302);
        yt[6][tid] = __builtin_amdgcn_perm(yb.w, ya.w, 0x05040100);
        yt[7][tid] = __builtin_amdgcn_perm(yb.w, ya.w, 0x07060302);
    }
    u32 l1p[5];
#pragma unroll
    for (int i = 0; i < 10; i += 2) {
        u32 dd = pk2(x[i] - m1, x[i + 1] - m1);
        l1p[i >> 1] = pk2(lo2f(dd) * r1, hi2f(dd) * r1);
    }
    __syncthreads();
    STAMP(2, 2);
    const u32x4 y4 = *(const u32x4*)(&yt[w][4 * lane]);
    const u32 y1 = yt[w][256 + lane];
    float y[10];
    unpack10(y4, y1, y);
    float h[10];
    u32 hp[5], op[5];
#pragma unroll
    for (int i = 0; i < 10; i += 2) {
        u32 l1 = l1p[i >> 1];
        u32 a = pk2(lo2f(l1) * y[i], hi2f(l1) * y[i + 1]);
        u32 hh = pk2(x[i] + lo2f(a), x[i + 1] + hi2f(a));
        hp[i >> 1] = hh;
        h[i] = lo2f(hh);
        h[i + 1] = hi2f(hh);
    }
    *(u32x4*)(hbuf + (size_t)t * D + 8 * lane) = (u32x4){hp[0], hp[1], hp[2], hp[3]};
    *(u32*)(hbuf + (size_t)t * D + 512 + 2 * lane) = hp[4];
    float m2, s2;
    ln_stats(h, m2, s2);
    const float r2 = 1.0f / s2;
    float wr[10];
    unpack10(wa, wb, wr);
    float racc0 = 0.f, racc1 = 0.f;
#pragma unroll
    for (int i = 0; i < 10; i += 2) {
        u32 dd = pk2(h[i] - m2, h[i + 1] - m2);
        u32 l2 = pk2(lo2f(dd) * r2, hi2f(dd) * r2);
        racc0 += lo2f(l2) * wr[i];
        racc1 += hi2f(l2) * wr[i + 1];
        op[i >> 1] = pk2(h[i] + lo2f(l2), h[i + 1] + hi2f(l2));
    }
    *(u32x4*)(out + (size_t)t * D + 8 * lane) = (u32x4){op[0], op[1], op[2], op[3]};
    *(u32*)(out + (size_t)t * D + 512 + 2 * lane) = op[4];
    const float racc = wave_sum(racc0 + racc1);
    STAMP(2, 3);
    if (lane == 0) {
        u32 rb = pk2(racc, 0.f) & 0xffffu;
        u32 ord = (rb & 0x8000u) ? (rb ^ 0xffffu) : (rb ^ 0x8000u);
        keys[t] = (ord << 16) | (u32)(0xffff - t);
        *(f32x2*)(stats + 2 * t) = (f32x2){m2, r2};
    }
    STAMP(2, 4);
}

__device__ __forceinline__ float key_r(u32 key) {
    u32 ord = key >> 16;
    u32 rb = (ord & 0x8000u) ? (ord ^ 0x8000u) : (ord ^ 0xffffu);
    return __uint_as_float(rb << 16);
}

constexpr int XS = 648;
__global__ __launch_bounds__(256) void k3_gateup(const u32* __restrict__ keys, const u16* __restrict__ hbuf,
                                                 const float* __restrict__ stats, const u16* __restrict__ wgF,
                                                 const u16* __restrict__ wuF, u16* __restrict__ gu,
                                                 u32* __restrict__ selkeys, u16* __restrict__ selh,
                                                 u16* __restrict__ sell2) {
    __shared__ __attribute__((aligned(16))) u16 xs[8 * XS];
    __shared__ __attribute__((aligned(16))) float red[4][2][8][16];
    __shared__ u32 cand[32];
    const int b = blockIdx.x;
    const int slice = b;
    const int f0 = slice * 16;
    const int tid = threadIdx.x, w = tid >> 6, lane = tid & 63;
    STAMP(3, 0);
    u32 a[8];
    {
        u32x4 k0 = *(const u32x4*)(keys + tid * 8);
        u32x4 k1 = *(const u32x4*)(keys + tid * 8 + 4);
        a[0] = k0.x; a[1] = k0.y; a[2] = k0.z; a[3] = k0.w; a[4] = k1.x; a[5] = k1.y; a[6] = k1.z; a[7] = k1.w;
    }
    bf16x8 bg[5], bu[5];
#pragma unroll
    for (int i = 0; i < 5; ++i) {
        bg[i] = *(const bf16x8*)(wgF + ((size_t)(slice * 20 + 5 * w + i) * 64 + lane) * 8);
        bu[i] = *(const bf16x8*)(wuF + ((size_t)(slice * 20 + 5 * w + i) * 64 + lane) * 8);
    }
#define CE(i, j) { u32 hi_ = a[i] > a[j] ? a[i] : a[j]; u32 lo_ = a[i] > a[j] ? a[j] : a[i]; a[i] = hi_; a[j] = lo_; }
    CE(0, 1) CE(2, 3) CE(4, 5) CE(6, 7)
    CE(0, 2) CE(1, 3) CE(4, 6) CE(5, 7)
    CE(1, 2) CE(5, 6) CE(0, 4) CE(3, 7)
    CE(1, 5) CE(2, 6)
    CE(1, 4) CE(3, 6)
    CE(2, 4) CE(3, 5)
    CE(3, 4)
#undef CE
    u32 mine = 0;
#pragma unroll
    for (int rd = 0; rd < 8; ++rd) {
        u32 m = wave_max_u32(a[0]);
        bool win = (a[0] == m);
#pragma unroll
        for (int i = 0; i < 7 - rd; ++i) a[i] = win ? a[i + 1] : a[i];
        if (lane == rd) mine = m;
    }
    if (lane < 8) cand[w * 8 + lane] = mine;
    STAMP(3, 1);
    __syncthreads();
    STAMP(3, 2);
    {
        u32 c = (lane < 32) ? cand[lane] : 0u;
        u32 k0 = 0, k1 = 0, mysel = 0;
#pragma unroll
        for (int rd = 0; rd < 8; ++rd) {
            u32 m = wave_max_u32(c);
            c = (c == m) ? 0u : c;
            if (lane == rd) mysel = m;
            if (rd == 2 * w) k0 = m;
            if (rd == 2 * w + 1) k1 = m;
        }
        const int ta = 0xffff - (int)(k0 & 0xffffu), tbb = 0xffff - (int)(k1 & 0xffffu);
        const u32x4 ha0 = *(const u32x4*)(hbuf + (size_t)ta * D + 8 * lane);
        const u32 hb0 = *(const u32*)(hbuf + (size_t)ta * D + 512 + 2 * lane);
        const u32x4 ha1 = *(const u32x4*)(hbuf + (size_t)tbb * D + 8 * lane);
        const u32 hb1 = *(const u32*)(hbuf + (size_t)tbb * D + 512 + 2 * lane);
        const f32x2 st0 = *(const f32x2*)(stats + 2 * ta);
        const f32x2 st1 = *(const f32x2*)(stats + 2 * tbb);
        STAMP(3, 3);
#pragma unroll
        for (int j = 0; j < 2; ++j) {
            const int tok = 2 * w + j;
            const u32x4 ha = j ? ha1 : ha0;
            const u32 hb = j ? hb1 : hb0;
            const float m2 = j ? st1.x : st0.x, r2 = j ? st1.y : st0.y;
            float h[10];
            unpack10(ha, hb, h);
            u32 l2[5];
#pragma unroll
            for (int i = 0; i < 10; i += 2) {
                u32 dd = pk2(h[i] - m2, h[i + 1] - m2);
                l2[i >> 1] = pk2(lo2f(dd) * r2, hi2f(dd) * r2);
            }
            *(u32x4*)(xs + tok * XS + 8 * lane) = (u32x4){l2[0], l2[1], l2[2], l2[3]};
            *(u32*)(xs + tok * XS + 512 + 2 * lane) = l2[4];
            if (b == 0) {
                *(u32x4*)(selh + tok * D + 8 * lane) = ha;
                *(u32*)(selh + tok * D + 512 + 2 * lane) = hb;
                *(u32x4*)(sell2 + tok * D + 8 * lane) = (u32x4){l2[0], l2[1], l2[2], l2[3]};
                *(u32*)(sell2 + tok * D + 512 + 2 * lane) = l2[4];
            }
        }
        if (b == 0 && w == 0 && lane < 8) selkeys[lane] = mysel;
    }
    STAMP(3, 4);
    __syncthreads();
    STAMP(3, 5);
    {
        const int li = lane & 15, g = lane >> 4;
        f32x4 ag = {0.f, 0.f, 0.f, 0.f}, au = {0.f, 0.f, 0.f, 0.f};
#pragma unroll
        for (int s = 0; s < 5; ++s) {
            const int k0 = (5 * w + s) * 32;
            bf16x8 af = *(const bf16x8*)(xs + (li & 7) * XS + k0 + 8 * g);
            ag = __builtin_amdgcn_mfma_f32_16x16x32_bf16(af, bg[s], ag, 0, 0, 0);
            au = __builtin_amdgcn_mfma_f32_16x16x32_bf16(af, bu[s], au, 0, 0, 0);
        }
        if (g < 2) {
#pragma unroll
            for (int rr = 0; rr < 4; ++rr) {
                red[w][0][4 * g + rr][li] = ag[rr];
                red[w][1][4 * g + rr][li] = au[rr];
            }
        }
    }
    STAMP(3, 6);
    __syncthreads();
    if (tid < 128) {
        const int tok = tid >> 4, col = tid & 15;
        float gs = (red[0][0][tok][col] + red[1][0][tok][col]) + (red[2][0][tok][col] + red[3][0][tok][col]);
        float us = (red[0][1][tok][col] + red[1][1][tok][col]) + (red[2][1][tok][col] + red[3][1][tok][col]);
        float gb = rbf(gs), ub = rbf(us);
        float sg = rbf(gb / (1.0f + __expf(-gb)));
        u32 o = pk2(sg * ub, 0.f);
        const int f = f0 + col;
        gu[((f >> 5) * 4 + ((f >> 3) & 3)) * 64 + tok * 8 + (f & 7)] = (u16)(o & 0xffffu);
    }
    STAMP(3, 7);
}

__global__ __launch_bounds__(512) void k4_down(const u16* __restrict__ gu, const u16* __restrict__ wdF,
                                               const u32* __restrict__ selkeys, const u16* __restrict__ selh,
                                               const u16* __restrict__ sell2, u16* __restrict__ out) {
    __shared__ __attribute__((aligned(16))) float red[8][8][16];
    const int slice = blockIdx.x;
    const int d0 = slice * 8;
    const int tid = threadIdx.x, w = tid >> 6, lane = tid & 63;
    const int li = lane & 15, g = lane >> 4;
    STAMP(4, 0);
    bf16x8 af[7], bf[7];
#pragma unroll
    for (int j = 0; j < 7; ++j) {
        int s = w + 8 * j;
        if (s < 54) {
            af[j] = *(const bf16x8*)(gu + (s * 4 + g) * 64 + (li & 7) * 8);
            bf[j] = *(const bf16x8*)(wdF + ((size_t)(slice * 54 + s) * 32 + g * 8 + (li & 7)) * 8);
        }
    }
    float hv = 0.f, l2v = 0.f;
    u32 key = 0;
    if (tid < 64) {
        const int tok = tid >> 3, col = tid & 7;
        key = selkeys[tok];
        hv = __uint_as_float(((u32)selh[tok * D + d0 + col]) << 16);
        l2v = __uint_as_float(((u32)sell2[tok * D + d0 + col]) << 16);
    }
    STAMP(4, 1);
    {
        f32x4 acc = {0.f, 0.f, 0.f, 0.f};
#pragma unroll
        for (int j = 0; j < 7; ++j) {
            int s = w + 8 * j;
            if (s < 54) acc = __builtin_amdgcn_mfma_f32_16x16x32_bf16(af[j], bf[j], acc, 0, 0, 0);
        }
        if (g < 2) {
#pragma unroll
            for (int rr = 0; rr < 4; ++rr) red[w][4 * g + rr][li] = acc[rr];
        }
    }
    STAMP(4, 2);
    __syncthreads();
    if (tid < 64) {
        const int tok = tid >> 3, col = tid & 7;
        const int trow = 0xffff - (int)(key & 0xffffu);
        const float rv = key_r(key);
        const float rs = rbf(1.0f / (1.0f + __expf(-rv)));
        float s = ((red[0][tok][col] + red[1][tok][col]) + (red[2][tok][col] + red[3][tok][col])) +
                  ((red[4][tok][col] + red[5][tok][col]) + (red[6][tok][col] + red[7][tok][col]));
        float y = rbf(s);
        y = rbf(y * rs);
        float o1 = rbf(l2v + y);
        u32 o = pk2(hv + o1, 0.f);
        out[(size_t)trow * D + d0 + col] = (u16)(o & 0xffffu);
    }
    STAMP(4, 3);
}

extern "C" void kernel_launch(void* const* d_in, const int* in_sizes, int n_in, void* d_out, int out_size,
                              void* d_ws, size_t ws_size, hipStream_t stream) {
    const u16* q = (const u16*)d_in[0];
    const u16* k = (const u16*)d_in[1];
    const u16* v = (const u16*)d_in[2];
    const u16* shift_k = (const u16*)d_in[3];
    const u16* ssm_k = (const u16*)d_in[4];
    const u16* ssm_D = (const u16*)d_in[5];
    const u16* w_r = (const u16*)d_in[7];
    const u16* w_gate = (const u16*)d_in[8];
    const u16* w_up = (const u16*)d_in[9];
    const u16* w_down = (const u16*)d_in[10];
    u16* out = (u16*)d_out;
    char* ws = (char*)d_ws;
    const size_t TS = (size_t)S * D * 2;
    u16* uT = (u16*)(ws);
    u16* y2T = (u16*)(ws + TS);
    u16* hbuf = (u16*)(ws + 2 * TS);
    char* sm = ws + 3 * TS;
    float* stats = (float*)(sm);
    u32* keys = (u32*)(sm + 16384);
    u32* selkeys = (u32*)(sm + 24576);
    u16* selh = (u16*)(sm + 24576 + 256);
    u16* sell2 = (u16*)(sm + 24576 + 256 + 10240);
    u16* gu = (u16*)(sm + 24576 + 256 + 20480);
    const size_t WS_ = (size_t)D * F * 2;
    u16* wgF = (u16*)(sm + 131072);
    u16* wuF = (u16*)(sm + 131072 + WS_);
    u16* wdF = (u16*)(sm + 131072 + 2 * WS_);
#define PROF_MASK 0xff
    if (PROF_MASK & 1) k0_kv<<<320, 512, 0, stream>>>(k, v, shift_k, uT, ssm_k, q);
    if (PROF_MASK & 2) k1_conv<<<K1_CONV_WGS + 405, 256, 0, stream>>>(uT, ssm_k, ssm_D, y2T, w_gate, w_up, w_down, wgF, wuF, wdF);
    if (PROF_MASK & 4) k2_row<<<256, 512, 0, stream>>>(q, y2T, w_r, out, hbuf, stats, keys);
    if (PROF_MASK & 8) k3_gateup<<<108, 256, 0, stream>>>(keys, hbuf, stats, wgF, wuF, gu, selkeys, selh, sell2);
    if (PROF_MASK & 16) k4_down<<<80, 512, 0, stream>>>(gu, wdF, selkeys, selh, sell2, out);
}
```

```cpp
#include <hip/hip_runtime.h>
#include <hip/hip_bf16.h>

typedef unsigned short u16;
typedef unsigned int u32;
typedef __attribute__((ext_vector_type(8))) short bf16x8;
typedef __attribute__((ext_vector_type(4))) short s16x4;
typedef __attribute__((ext_vector_type(4))) float f32x4;
typedef __attribute__((ext_vector_type(2))) float f32x2;
typedef __attribute__((ext_vector_type(2))) __bf16 bfx2;
typedef __attribute__((ext_vector_type(4))) unsigned u32x4;
typedef __attribute__((ext_vector_type(2))) unsigned u32x2;

constexpr int S = 2048, D = 640, F = 1728, NSEL = 8;

#define STAMP(kid, i)
#define STAMPN(kid, i)

__device__ __forceinline__ u32 pk2(float a, float b) {
    f32x2 v = {a, b};
    bfx2 r = __builtin_convertvector(v, bfx2);
    return __builtin_bit_cast(u32, r);
}
__device__ __forceinline__ float lo2f(u32 p) { return __uint_as_float(p << 16); }
__device__ __forceinline__ float hi2f(u32 p) { return __uint_as_float(p & 0xffff0000u); }
__device__ __forceinline__ float rbf(float a) { return lo2f(pk2(a, 0.f)); }

template <int CTRL, int RM> __device__ __forceinline__ int dpp_i(int v) {
    return __builtin_amdgcn_update_dpp(0, v, CTRL, RM, 0xf, true);
}
__device__ __forceinline__ float wave_sum(float v) {
    v += __int_as_float(dpp_i<0x111, 0xf>(__float_as_int(v)));
    v += __int_as_float(dpp_i<0x112, 0xf>(__float_as_int(v)));
    v += __int_as_float(dpp_i<0x114, 0xf>(__float_as_int(v)));
    v += __int_as_float(dpp_i<0x118, 0xf>(__float_as_int(v)));
    v += __int_as_float(dpp_i<0x142, 0xa>(__float_as_int(v)));
    v += __int_as_float(dpp_i<0x143, 0xc>(__float_as_int(v)));
    return __int_as_float(__builtin_amdgcn_readlane(__float_as_int(v), 63));
}
__device__ __forceinline__ u32 umax_(u32 a, u32 b) { return a > b ? a : b; }
__device__ __forceinline__ u32 wave_max_u32(u32 v) {
    v = umax_(v, (u32)dpp_i<0x111, 0xf>((int)v));
    v = umax_(v, (u32)dpp_i<0x112, 0xf>((int)v));
    v = umax_(v, (u32)dpp_i<0x114, 0xf>((int)v));
    v = umax_(v, (u32)dpp_i<0x118, 0xf>((int)v));
    v = umax_(v, (u32)dpp_i<0x142, 0xa>((int)v));
    v = umax_(v, (u32)dpp_i<0x143, 0xc>((int)v));
    return (u32)__builtin_amdgcn_readlane((int)v, 63);
}
#define KEEP4(val_) asm volatile("" ::"v"((val_).x), "v"((val_).y), "v"((val_).z), "v"((val_).w))

typedef __attribute__((address_space(3))) s16x4* lds_s16x4_ptr;
__device__ __forceinline__ s16x4 tr_read(const u16* p) {
    return __builtin_amdgcn_ds_read_tr16_b64_v4i16((lds_s16x4_ptr)p);
}
__device__ __forceinline__ u32 lds_addr(const u16* p) {
    return (u32)(size_t)(const __attribute__((address_space(3))) u16*)p;
}
__device__ __forceinline__ void wave_lds_fence() {
    __builtin_amdgcn_fence(__ATOMIC_RELEASE, "wavefront");
    __builtin_amdgcn_wave_barrier();
    __builtin_amdgcn_fence(__ATOMIC_ACQUIRE, "wavefront");
}

__global__ __launch_bounds__(512) void k0_kv(const u16* __restrict__ k, const u16* __restrict__ v,
                                             const u16* __restrict__ shift_k, u16* __restrict__ uT,
                                             const u16* __restrict__ pf0, const u16* __restrict__ pf1) {
    __shared__ __attribute__((aligned(16))) u16 tile[64 * 72];
    const int b = blockIdx.x;
    const int ct = b % 10, rt = b / 10;
    const int tid = threadIdx.x;
    const int cp = tid & 31, rg = tid >> 5;
    const int t0 = rt * 64 + rg * 4;
    const int c = ct * 32 + cp;
    STAMP(0, 0);
    u32 kk[7], vv[4];
#pragma unroll
    for (int i = 0; i < 7; ++i) {
        int t = t0 - 3 + i;
        kk[i] = (t >= 0) ? *(const u32*)(k + (size_t)t * D + 2 * c) : 0u;
    }
#pragma unroll
    for (int i = 0; i < 4; ++i) vv[i] = *(const u32*)(v + (size_t)(t0 + i) * D + 2 * c);
    const u32x4 sw = *(const u32x4*)(shift_k + 8 * c);
    const u32x4 pa = *(const u32x4*)(pf0 + (size_t)(b * 512 + tid) * 8);
    const u32x4 pb = *(const u32x4*)(pf1 + (size_t)(b * 512 + tid) * 8);
    const float a0 = lo2f(sw.x), a1 = hi2f(sw.x), a2 = lo2f(sw.y), a3 = hi2f(sw.y);
    const float b0 = lo2f(sw.z), b1 = hi2f(sw.z), b2 = lo2f(sw.w), b3 = hi2f(sw.w);
    float ua[4], ub[4];
#pragma unroll
    for (int i = 0; i < 4; ++i) {
        u32 p0 = pk2(lo2f(kk[i]) * a3, hi2f(kk[i]) * b3);
        u32 p1 = pk2(lo2f(kk[i + 1]) * a2, hi2f(kk[i + 1]) * b2);
        u32 p2 = pk2(lo2f(kk[i + 2]) * a1, hi2f(kk[i + 2]) * b1);
        u32 p3 = pk2(lo2f(kk[i + 3]) * a0, hi2f(kk[i + 3]) * b0);
        u32 s = pk2(lo2f(p0) + lo2f(p1), hi2f(p0) + hi2f(p1));
        s = pk2(lo2f(s) + lo2f(p2), hi2f(s) + hi2f(p2));
        s = pk2(lo2f(s) + lo2f(p3), hi2f(s) + hi2f(p3));
        ua[i] = lo2f(s) * lo2f(vv[i]);
        ub[i] = hi2f(s) * hi2f(vv[i]);
    }
    u32x2 oa, ob;
    oa.x = pk2(ua[3], ua[2]); oa.y = pk2(ua[1], ua[0]);
    ob.x = pk2(ub[3], ub[2]); ob.y = pk2(ub[1], ub[0]);
    *(u32x2*)(tile + (2 * cp) * 72 + ((rg * 4) ^ 4)) = oa;
    *(u32x2*)(tile + (2 * cp + 1) * 72 + ((rg * 4) ^ 4)) = ob;
    STAMP(0, 1);
    __syncthreads();
    {
        const int ch = tid >> 3, pc = tid & 7;
        const u32x4 o = *(const u32x4*)(tile + ch * 72 + pc * 8);
        *(u32x4*)(uT + (size_t)(ct * 64 + ch) * S + rt * 64 + pc * 8) = o;
    }
    KEEP4(pa);
    KEEP4(pb);
    STAMP(0, 2);
}

constexpr int UPAD = 256;
constexpr int KCS = 2080;
constexpr int WCH = 138240;

constexpr int K1_CONV_WGS = D;
#define K1_EXTRA 0
__global__ __launch_bounds__(256, 3) void k1_conv(const u16* __restrict__ uT, const u16* __restrict__ ssm_k,
                                               const u16* __restrict__ ssm_D, u16* __restrict__ y2T,
                                               const u16* __restrict__ w_gate, const u16* __restrict__ w_up,
                                               const u16* __restrict__ w_down, u16* __restrict__ wgF,
                                               u16* __restrict__ wuF, u16* __restrict__ wdF) {
    __shared__ __attribute__((aligned(16))) u16 lds[UPAD + S + 12288 + 3 * 2048];
    u16* uL = lds;
    u16* kL = lds + UPAD + S;
    f32x4* part = (f32x4*)(lds + UPAD + S);
    const int tid = threadIdx.x;
    const int w = __builtin_amdgcn_readfirstlane(tid >> 6), lane = tid & 63;
    const int d = blockIdx.x;
    const u16* urow = uT + (size_t)d * S;
    const u16* krow = ssm_k + (size_t)d * S;
    STAMP(1, 0);
    const u32x4 z4 = {0u, 0u, 0u, 0u};
    const u32x4 ur = *(const u32x4*)(urow + tid * 8);
    const u32x4 kd = *(const u32x4*)(krow + tid * 8);
    u32x2 ke = *(const u32x2*)(krow + (tid < 255 ? tid * 8 + 8 : 0));
    const u32 dpair = ((const u32*)ssm_D)[d >> 1];
    const float Dv = (d & 1) ? hi2f(dpair) : lo2f(dpair);
    if (tid == 255) ke = (u32x2){0u, 0u};
    *(u32x4*)(uL + UPAD + tid * 8) = ur;
    if (tid < 32) *(u32x4*)(uL + tid * 8) = z4;
    {
        u32 d0 = kd.x, d1 = kd.y, d2 = kd.z, d3 = kd.w, d4 = ke.x, d5 = ke.y;
        u32 s0 = __builtin_amdgcn_alignbit(d1, d0, 16);
        u32 s1 = __builtin_amdgcn_alignbit(d2, d1, 16);
        u32 s2 = __builtin_amdgcn_alignbit(d3, d2, 16);
        u32 s3 = __builtin_amdgcn_alignbit(d4, d3, 16);
        u32 s4 = __builtin_amdgcn_alignbit(d5, d4, 16);
        u32x4 c0 = {d0, d1, d2, d3}, c1 = {s0, s1, s2, s3}, c2 = {d1, d2, d3, d4}, c3 = {s1, s2, s3, s4};
        const int Y = tid + 2;
        *(u32x4*)(kL + 0 * KCS + Y * 8) = c0;
        *(u32x4*)(kL + 1 * KCS + Y * 8) = c1;
        *(u32x4*)(kL + 2 * KCS + Y * 8) = c2;
        *(u32x4*)(kL + 3 * KCS + Y * 8) = c3;
        if (tid == 0) {
            u32x4 e1 = {0u, 0u, 0u, d0 << 16}, e2 = {0u, 0u, 0u, d0}, e3 = {0u, 0u, d0 << 16, s0};
            *(u32x4*)(kL + 0 * KCS + 0) = z4; *(u32x4*)(kL + 0 * KCS + 8) = z4;
            *(u32x4*)(kL + 1 * KCS + 0) = z4; *(u32x4*)(kL + 1 * KCS + 8) = e1;
            *(u32x4*)(kL + 2 * KCS + 0) = z4; *(u32x4*)(kL + 2 * KCS + 8) = e2;
            *(u32x4*)(kL + 3 * KCS + 0) = z4; *(u32x4*)(kL + 3 * KCS + 8) = e3;
        }
    }
    const int T = w * D + d;
    const int tm = T / 540, trem = T - tm * 540;
    const int tks = (tm < 2) ? trem / 27 : trem / 10;
    const int tcg = (tm < 2) ? trem - tks * 27 : trem - tks * 10;
    u16* tl = lds + UPAD + S + 12288 + w * 2048;
    if (T < 1620) {
        const u16* src = (tm < 2) ? ((tm ? w_up : w_gate) + (size_t)(tks * 32) * F + tcg * 64)
                                  : (w_down + (size_t)(tks * 32) * D + tcg * 64);
        const int ldm = (tm < 2) ? F : D;
#pragma unroll
        for (int i = 0; i < 4; ++i) {
            const int ch = i * 64 + lane;
            const u16* gsrc = src + (size_t)(ch >> 3) * ldm + (ch & 7) * 8;
            const u32 ldst = __builtin_amdgcn_readfirstlane(lds_addr(tl) + i * 1024);
            u32 keep;
            asm volatile("s_mov_b32 %0, m0\n\ts_mov_b32 m0, %2\n\ts_nop 0\n\tglobal_load_lds_dwordx4 %1, off\n\ts_mov_b32 m0, %0"
                         : "=&s"(keep) : "v"(gsrc), "s"(ldst) : "memory");
        }
    }
    __syncthreads();
    STAMPN(1, 1);
    const int li = lane & 15, g = lane >> 4;
    const u16* aBase = kL + ((li + 1) & 3) * KCS + ((li + 1) & ~3) + 8 * g + 64 * w;
    const u16* bBase = uL + UPAD + 16 * li + 8 - 8 * g - 64 * w;
    f32x4 acc[8];
#pragma unroll
    for (int c = 0; c < 8; ++c) acc[c] = (f32x4){0.f, 0.f, 0.f, 0.f};
    const u32 aAddr = lds_addr(aBase);
    const u32 bAddr = lds_addr(bBase - 32);
    s16x4 alo[2][8], ahi[2][8];
    bf16x8 bf[2][8];
#define LOAD_FRAGS(r)                                                                                   \
    _Pragma("unroll") for (int n = 0; n < 8; ++n) {                                                     \
        asm volatile("ds_read_b64 %0, %1 offset:%2" : "=v"(alo[r][n]) : "v"(aAddr), "i"(64 * (8 * n + r))); \
        asm volatile("ds_read_b64 %0, %1 offset:%2" : "=v"(ahi[r][n]) : "v"(aAddr), "i"(64 * (8 * n + r) + 8)); \
        asm volatile("ds_read_b128 %0, %1 offset:%2" : "=v"(bf[r][n]) : "v"(bAddr), "i"(64 * (8 * n - r) + 64)); \
    }
#define MFMA_ALL(r)                                                                                     \
    _Pragma("unroll") for (int sum = 0; sum < 8; ++sum) _Pragma("unroll") for (int n = 0; n <= sum; ++n) \
        acc[sum] = __builtin_amdgcn_mfma_f32_16x16x32_bf16(                                             \
            __builtin_shufflevector(alo[r][n], ahi[r][n], 0, 1, 2, 3, 4, 5, 6, 7), bf[r][sum - n], acc[sum], 0, 0, 0);
    LOAD_FRAGS(0)
    asm volatile("s_waitcnt lgkmcnt(0)" ::: "memory");
    __builtin_amdgcn_sched_barrier(0);
    LOAD_FRAGS(1)
    __builtin_amdgcn_sched_barrier(0);
    MFMA_ALL(0)
    __builtin_amdgcn_sched_barrier(0);
    asm volatile("s_waitcnt lgkmcnt(0)" ::: "memory");
    __builtin_amdgcn_sched_barrier(0);
    MFMA_ALL(1)
#undef LOAD_FRAGS
#undef MFMA_ALL
    STAMPN(1, 2);
    __syncthreads();
#pragma unroll
    for (int c = 0; c < 8; ++c) {
        const int o = c >> 1;
        if (o != w) part[((o * 3 + ((w - o - 1) & 3)) * 2 + (c & 1)) * 64 + lane] = acc[c];
    }
    __syncthreads();
    STAMPN(1, 3);
    u16* yrow = y2T + (size_t)d * S;
#pragma unroll
    for (int j = 0; j < 2; ++j) {
        const int c = 2 * w + j;
        f32x4 own = acc[j];
#pragma unroll
        for (int q = 1; q < 4; ++q) own = (w == q) ? acc[2 * q + j] : own;
        f32x4 a0 = part[((w * 3 + 0) * 2 + j) * 64 + lane], a1 = part[((w * 3 + 1) * 2 + j) * 64 + lane];
        f32x4 a2 = part[((w * 3 + 2) * 2 + j) * 64 + lane];
        f32x4 a = (a0 + a1) + (a2 + own);
        const int tb = 256 * c + 16 * li + 8 * (g >> 1);
        u32x2 uu = *(const u32x2*)(uL + UPAD + tb + 4 * (1 - (g & 1)));
        float u3 = lo2f(uu.x), u2 = hi2f(uu.x), u1 = lo2f(uu.y), u0 = hi2f(uu.y);
        u32 y01 = pk2(a[0], a[1]);
        u32 y23 = pk2(a[2], a[3]);
        u32 w01 = pk2(u0 * Dv, u1 * Dv);
        u32 w23 = pk2(u2 * Dv, u3 * Dv);
        u32x2 o;
        o.x = pk2(lo2f(y01) + lo2f(w01), hi2f(y01) + hi2f(w01));
        o.y = pk2(lo2f(y23) + lo2f(w23), hi2f(y23) + hi2f(w23));
        *(u32x2*)(yrow + 256 * c + 16 * li + 4 * g) = o;
    }
    STAMPN(1, 4);
    if (T < 1620) {
        asm volatile("s_waitcnt vmcnt(0)" ::: "memory");
#pragma unroll
        for (int j = 0; j < 4; ++j) {
            const u16* rp = tl + (8 * g + (li >> 2)) * 64 + 16 * j + 4 * (li & 3);
            s16x4 b0 = tr_read(rp), b1 = tr_read(rp + 4 * 64);
            bf16x8 fr = __builtin_shufflevector(b0, b1, 0, 1, 2, 3, 4, 5, 6, 7);
            u16* dst;
            if (tm < 2) dst = (tm ? wuF : wgF) + ((size_t)((tcg * 4 + j) * 20 + tks) * 64 + lane) * 8;
            else dst = wdF + ((size_t)((tcg * 8 + 2 * j + (li >> 3)) * 54 + tks) * 32 + g * 8 + (li & 7)) * 8;
            asm volatile("global_store_dwordx4 %0, %1, off sc1\n\ts_nop 1" ::"v"(dst), "v"(fr) : "memory");
        }
    }
    STAMP(1, 5);
}

__device__ __forceinline__ void unpack10(const u32x4& a, u32 b, float* f) {
    f[0] = lo2f(a.x); f[1] = hi2f(a.x); f[2] = lo2f(a.y); f[3] = hi2f(a.y);
    f[4] = lo2f(a.z); f[5] = hi2f(a.z); f[6] = lo2f(a.w); f[7] = hi2f(a.w);
    f[8] = lo2f(b); f[9] = hi2f(b);
}
__device__ __forceinline__ void ln_stats(const float* x, float& m_bf, float& s_bf) {
    float s = ((x[0] + x[1]) + (x[2] + x[3])) + ((x[4] + x[5]) + (x[6] + x[7])) + (x[8] + x[9]);
    s = wave_sum(s);
    const float mean = s * (1.0f / D);
    float q0 = 0.f, q1 = 0.f;
#pragma unroll
    for (int i = 0; i < 10; i += 2) {
        float d0 = x[i] - mean, d1 = x[i + 1] - mean;
        q0 += d0 * d0;
        q1 += d1 * d1;
    }
    float q = wave_sum(q0 + q1);
    const float var = q * (1.0f / D);
    m_bf = rbf(mean);
    float vb = rbf(var);
    float ve = rbf(vb + 1e-5f);
    s_bf = rbf(sqrtf(ve));
}

__global__ __launch_bounds__(512) void k2_row(const u16* __restrict__ q, const u16* __restrict__ y2T,
                                              const u16* __restrict__ w_r, u16* __restrict__ out,
                                              u16* __restrict__ hbuf, float* __restrict__ stats,
                                              u32* __restrict__ keys) {
    __shared__ __attribute__((aligned(16))) u32 yt[8][320];
    const int b = blockIdx.x;
    const int tb = (b & 7) * 32 + (b >> 3);
    const int t0 = tb * 8;
    const int tid = threadIdx.x;
    const int w = tid >> 6, lane = tid & 63;
    const int t = t0 + w;
    STAMP(2, 0);
    const u32x4 xa = *(const u32x4*)(q + (size_t)t * D + 8 * lane);
    const u32 xb = *(const u32*)(q + (size_t)t * D + 512 + 2 * lane);
    u32x4 ya = {0u, 0u, 0u, 0u}, yb = {0u, 0u, 0u, 0u};
    if (tid < 320) {
        ya = *(const u32x4*)(y2T + (size_t)(2 * tid) * S + t0);
        yb = *(const u32x4*)(y2T + (size_t)(2 * tid + 1) * S + t0);
    }
    const u32x4 wa = *(const u32x4*)(w_r + 8 * lane);
    const u32 wb = *(const u32*)(w_r + 512 + 2 * lane);
    float x[10];
    unpack10(xa, xb, x);
    float m1, s1;
    ln_stats(x, m1, s1);
    const float r1 = 1.0f / s1;
    STAMP(2, 1);
    if (tid < 320) {
        yt[0][tid] = __builtin_amdgcn_perm(yb.x, ya.x, 0x05040100);
        yt[1][tid] = __builtin_amdgcn_perm(yb.x, ya.x, 0x07060302);
        yt[2][tid] = __builtin_amdgcn_perm(yb.y, ya.y, 0x05040100);
        yt[3][tid] = __builtin_amdgcn_perm(yb.y, ya.y, 0x07060302);
        yt[4][tid] = __builtin_amdgcn_perm(yb.z, ya.z, 0x05040100);
        yt[5][tid] = __builtin_amdgcn_perm(yb.z, ya.z, 0x07060302);
        yt[6][tid] = __builtin_amdgcn_perm(yb.w, ya.w, 0x05040100);
        yt[7][tid] = __builtin_amdgcn_perm(yb.w, ya.w, 0x07060302);
    }
    u32 l1p[5];
#pragma unroll
    for (int i = 0; i < 10; i += 2) {
        u32 dd = pk2(x[i] - m1, x[i + 1] - m1);
        l1p[i >> 1] = pk2(lo2f(dd) * r1, hi2f(dd) * r1);
    }
    __syncthreads();
    STAMP(2, 2);
    const u32x4 y4 = *(const u32x4*)(&yt[w][4 * lane]);
    const u32 y1 = yt[w][256 + lane];
    float y[10];
    unpack10(y4, y1, y);
    float h[10];
    u32 hp[5], op[5];
#pragma unroll
    for (int i = 0; i < 10; i += 2) {
        u32 l1 = l1p[i >> 1];
        u32 a = pk2(lo2f(l1) * y[i], hi2f(l1) * y[i + 1]);
        u32 hh = pk2(x[i] + lo2f(a), x[i + 1] + hi2f(a));
        hp[i >> 1] = hh;
        h[i] = lo2f(hh);
        h[i + 1] = hi2f(hh);
    }
    *(u32x4*)(hbuf + (size_t)t * D + 8 * lane) = (u32x4){hp[0], hp[1], hp[2], hp[3]};
    *(u32*)(hbuf + (size_t)t * D + 512 + 2 * lane) = hp[4];
    float m2, s2;
    ln_stats(h, m2, s2);
    const float r2 = 1.0f / s2;
    float wr[10];
    unpack10(wa, wb, wr);
    float racc0 = 0.f, racc1 = 0.f;
#pragma unroll
    for (int i = 0; i < 10; i += 2) {
        u32 dd = pk2(h[i] - m2, h[i + 1] - m2);
        u32 l2 = pk2(lo2f(dd) * r2, hi2f(dd) * r2);
        racc0 += lo2f(l2) * wr[i];
        racc1 += hi2f(l2) * wr[i + 1];
        op[i >> 1] = pk2(h[i] + lo2f(l2), h[i + 1] + hi2f(l2));
    }
    *(u32x4*)(out + (size_t)t * D + 8 * lane) = (u32x4){op[0], op[1], op[2], op[3]};
    *(u32*)(out + (size_t)t * D + 512 + 2 * lane) = op[4];
    const float racc = wave_sum(racc0 + racc1);
    STAMP(2, 3);
    if (lane == 0) {
        u32 rb = pk2(racc, 0.f) & 0xffffu;
        u32 ord = (rb & 0x8000u) ? (rb ^ 0xffffu) : (rb ^ 0x8000u);
        keys[t] = (ord << 16) | (u32)(0xffff - t);
        *(f32x2*)(stats + 2 * t) = (f32x2){m2, r2};
    }
    STAMP(2, 4);
}

__device__ __forceinline__ float key_r(u32 key) {
    u32 ord = key >> 16;
    u32 rb = (ord & 0x8000u) ? (ord ^ 0x8000u) : (ord ^ 0xffffu);
    return __uint_as_float(rb << 16);
}

constexpr int XS = 648;
__global__ __launch_bounds__(256) void k3_gateup(const u32* __restrict__ keys, const u16* __restrict__ hbuf,
                                                 const float* __restrict__ stats, const u16* __restrict__ wgF,
                                                 const u16* __restrict__ wuF, u16* __restrict__ gu,
                                                 u32* __restrict__ selkeys, u16* __restrict__ selh,
                                                 u16* __restrict__ sell2) {
    __shared__ __attribute__((aligned(16))) u16 xs[8 * XS];
    __shared__ __attribute__((aligned(16))) float red[4][2][8][16];
    __shared__ u32 cand[32];
    const int b = blockIdx.x;
    const int slice = b;
    const int f0 = slice * 16;
    const int tid = threadIdx.x, w = tid >> 6, lane = tid & 63;
    STAMP(3, 0);
    u32 a[8];
    {
        u32x4 k0 = *(const u32x4*)(keys + tid * 8);
        u32x4 k1 = *(const u32x4*)(keys + tid * 8 + 4);
        a[0] = k0.x; a[1] = k0.y; a[2] = k0.z; a[3] = k0.w; a[4] = k1.x; a[5] = k1.y; a[6] = k1.z; a[7] = k1.w;
    }
    bf16x8 bg[5], bu[5];
#pragma unroll
    for (int i = 0; i < 5; ++i) {
        bg[i] = *(const bf16x8*)(wgF + ((size_t)(slice * 20 + 5 * w + i) * 64 + lane) * 8);
        bu[i] = *(const bf16x8*)(wuF + ((size_t)(slice * 20 + 5 * w + i) * 64 + lane) * 8);
    }
#define CE(i, j) { u32 hi_ = a[i] > a[j] ? a[i] : a[j]; u32 lo_ = a[i] > a[j] ? a[j] : a[i]; a[i] = hi_; a[j] = lo_; }
    CE(0, 1) CE(2, 3) CE(4, 5) CE(6, 7)
    CE(0, 2) CE(1, 3) CE(4, 6) CE(5, 7)
    CE(1, 2) CE(5, 6) CE(0, 4) CE(3, 7)
    CE(1, 5) CE(2, 6)
    CE(1, 4) CE(3, 6)
    CE(2, 4) CE(3, 5)
    CE(3, 4)
#undef CE
    u32 mine = 0;
#pragma unroll
    for (int rd = 0; rd < 8; ++rd) {
        u32 m = wave_max_u32(a[0]);
        bool win = (a[0] == m);
#pragma unroll
        for (int i = 0; i < 7 - rd; ++i) a[i] = win ? a[i + 1] : a[i];
        if (lane == rd) mine = m;
    }
    if (lane < 8) cand[w * 8 + lane] = mine;
    STAMP(3, 1);
    __syncthreads();
    STAMP(3, 2);
    {
        u32 c = (lane < 32) ? cand[lane] : 0u;
        u32 k0 = 0, k1 = 0, mysel = 0;
#pragma unroll
        for (int rd = 0; rd < 8; ++rd) {
            u32 m = wave_max_u32(c);
            c = (c == m) ? 0u : c;
            if (lane == rd) mysel = m;
            if (rd == 2 * w) k0 = m;
            if (rd == 2 * w + 1) k1 = m;
        }
        const int ta = 0xffff - (int)(k0 & 0xffffu), tbb = 0xffff - (int)(k1 & 0xffffu);
        const u32x4 ha0 = *(const u32x4*)(hbuf + (size_t)ta * D + 8 * lane);
        const u32 hb0 = *(const u32*)(hbuf + (size_t)ta * D + 512 + 2 * lane);
        const u32x4 ha1 = *(const u32x4*)(hbuf + (size_t)tbb * D + 8 * lane);
        const u32 hb1 = *(const u32*)(hbuf + (size_t)tbb * D + 512 + 2 * lane);
        const f32x2 st0 = *(const f32x2*)(stats + 2 * ta);
        const f32x2 st1 = *(const f32x2*)(stats + 2 * tbb);
        STAMP(3, 3);
#pragma unroll
        for (int j = 0; j < 2; ++j) {
            const int tok = 2 * w + j;
            const u32x4 ha = j ? ha1 : ha0;
            const u32 hb = j ? hb1 : hb0;
            const float m2 = j ? st1.x : st0.x, r2 = j ? st1.y : st0.y;
            float h[10];
            unpack10(ha, hb, h);
            u32 l2[5];
#pragma unroll
            for (int i = 0; i < 10; i += 2) {
                u32 dd = pk2(h[i] - m2, h[i + 1] - m2);
                l2[i >> 1] = pk2(lo2f(dd) * r2, hi2f(dd) * r2);
            }
            *(u32x4*)(xs + tok * XS + 8 * lane) = (u32x4){l2[0], l2[1], l2[2], l2[3]};
            *(u32*)(xs + tok * XS + 512 + 2 * lane) = l2[4];
            if (b == 0) {
                *(u32x4*)(selh + tok * D + 8 * lane) = ha;
                *(u32*)(selh + tok * D + 512 + 2 * lane) = hb;
                *(u32x4*)(sell2 + tok * D + 8 * lane) = (u32x4){l2[0], l2[1], l2[2], l2[3]};
                *(u32*)(sell2 + tok * D + 512 + 2 * lane) = l2[4];
            }
        }
        if (b == 0 && w == 0 && lane < 8) selkeys[lane] = mysel;
    }
    STAMP(3, 4);
    __syncthreads();
    STAMP(3, 5);
    {
        const int li = lane & 15, g = lane >> 4;
        f32x4 ag = {0.f, 0.f, 0.f, 0.f}, au = {0.f, 0.f, 0.f, 0.f};
#pragma unroll
        for (int s = 0; s < 5; ++s) {
            const int k0 = (5 * w + s) * 32;
            bf16x8 af = *(const bf16x8*)(xs + (li & 7) * XS + k0 + 8 * g);
            ag = __builtin_amdgcn_mfma_f32_16x16x32_bf16(af, bg[s], ag, 0, 0, 0);
            au = __builtin_amdgcn_mfma_f32_16x16x32_bf16(af, bu[s], au, 0, 0, 0);
        }
        if (g < 2) {
#pragma unroll
            for (int rr = 0; rr < 4; ++rr) {
                red[w][0][4 * g + rr][li] = ag[rr];
                red[w][1][4 * g + rr][li] = au[rr];
            }
        }
    }
    STAMP(3, 6);
    __syncthreads();
    if (tid < 128) {
        const int tok = tid >> 4, col = tid & 15;
        float gs = (red[0][0][tok][col] + red[1][0][tok][col]) + (red[2][0][tok][col] + red[3][0][tok][col]);
        float us = (red[0][1][tok][col] + red[1][1][tok][col]) + (red[2][1][tok][col] + red[3][1][tok][col]);
        float gb = rbf(gs), ub = rbf(us);
        float sg = rbf(gb / (1.0f + __expf(-gb)));
        u32 o = pk2(sg * ub, 0.f);
        const int f = f0 + col;
        gu[((f >> 5) * 4 + ((f >> 3) & 3)) * 64 + tok * 8 + (f & 7)] = (u16)(o & 0xffffu);
    }
    STAMP(3, 7);
}

__global__ __launch_bounds__(512) void k4_down(const u16* __restrict__ gu, const u16* __restrict__ wdF,
                                               const u32* __restrict__ selkeys, const u16* __restrict__ selh,
                                               const u16* __restrict__ sell2, u16* __restrict__ out) {
    __shared__ __attribute__((aligned(16))) float red[8][8][16];
    const int slice = blockIdx.x;
    const int d0 = slice * 8;
    const int tid = threadIdx.x, w = tid >> 6, lane = tid & 63;
    const int li = lane & 15, g = lane >> 4;
    STAMP(4, 0);
    bf16x8 af[7], bf[7];
#pragma unroll
    for (int j = 0; j < 7; ++j) {
        int s = w + 8 * j;
        if (s < 54) {
            af[j] = *(const bf16x8*)(gu + (s * 4 + g) * 64 + (li & 7) * 8);
            bf[j] = *(const bf16x8*)(wdF + ((size_t)(slice * 54 + s) * 32 + g * 8 + (li & 7)) * 8);
        }
    }
    float hv = 0.f, l2v = 0.f;
    u32 key = 0;
    if (tid < 64) {
        const int tok = tid >> 3, col = tid & 7;
        key = selkeys[tok];
        hv = __uint_as_float(((u32)selh[tok * D + d0 + col]) << 16);
        l2v = __uint_as_float(((u32)sell2[tok * D + d0 + col]) << 16);
    }
    STAMP(4, 1);
    {
        f32x4 acc = {0.f, 0.f, 0.f, 0.f};
#pragma unroll
        for (int j = 0; j < 7; ++j) {
            int s = w + 8 * j;
            if (s < 54) acc = __builtin_amdgcn_mfma_f32_16x16x32_bf16(af[j], bf[j], acc, 0, 0, 0);
        }
        if (g < 2) {
#pragma unroll
            for (int rr = 0; rr < 4; ++rr) red[w][4 * g + rr][li] = acc[rr];
        }
    }
    STAMP(4, 2);
    __syncthreads();
    if (tid < 64) {
        const int tok = tid >> 3, col = tid & 7;
        const int trow = 0xffff - (int)(key & 0xffffu);
        const float rv = key_r(key);
        const float rs = rbf(1.0f / (1.0f + __expf(-rv)));
        float s = ((red[0][tok][col] + red[1][tok][col]) + (red[2][tok][col] + red[3][tok][col])) +
                  ((red[4][tok][col] + red[5][tok][col]) + (red[6][tok][col] + red[7][tok][col]));
        float y = rbf(s);
        y = rbf(y * rs);
        float o1 = rbf(l2v + y);
        u32 o = pk2(hv + o1, 0.f);
        out[(size_t)trow * D + d0 + col] = (u16)(o & 0xffffu);
    }
    STAMP(4, 3);
}

extern "C" void kernel_launch(void* const* d_in, const int* in_sizes, int n_in, void* d_out, int out_size,
                              void* d_ws, size_t ws_size, hipStream_t stream) {
    const u16* q = (const u16*)d_in[0];
    const u16* k = (const u16*)d_in[1];
    const u16* v = (const u16*)d_in[2];
    const u16* shift_k = (const u16*)d_in[3];
    const u16* ssm_k = (const u16*)d_in[4];
    const u16* ssm_D = (const u16*)d_in[5];
    const u16* w_r = (const u16*)d_in[7];
    const u16* w_gate = (const u16*)d_in[8];
    const u16* w_up = (const u16*)d_in[9];
    const u16* w_down = (const u16*)d_in[10];
    u16* out = (u16*)d_out;
    char* ws = (char*)d_ws;
    const size_t TS = (size_t)S * D * 2;
    u16* uT = (u16*)(ws);
    u16* y2T = (u16*)(ws + TS);
    u16* hbuf = (u16*)(ws + 2 * TS);
    char* sm = ws + 3 * TS;
    float* stats = (float*)(sm);
    u32* keys = (u32*)(sm + 16384);
    u32* selkeys = (u32*)(sm + 24576);
    u16* selh = (u16*)(sm + 24576 + 256);
    u16* sell2 = (u16*)(sm + 24576 + 256 + 10240);
    u16* gu = (u16*)(sm + 24576 + 256 + 20480);
    const size_t WS_ = (size_t)D * F * 2;
    u16* wgF = (u16*)(sm + 131072);
    u16* wuF = (u16*)(sm + 131072 + WS_);
    u16* wdF = (u16*)(sm + 131072 + 2 * WS_);
#define PROF_MASK 0xff
    if (PROF_MASK & 1) k0_kv<<<320, 512, 0, stream>>>(k, v, shift_k, uT, ssm_k, q);
    if (PROF_MASK & 2) k1_conv<<<K1_CONV_WGS + K1_EXTRA, 256, 0, stream>>>(uT, ssm_k, ssm_D, y2T, w_gate, w_up, w_down, wgF, wuF, wdF);
    if (PROF_MASK & 4) k2_row<<<256, 512, 0, stream>>>(q, y2T, w_r, out, hbuf, stats, keys);
    if (PROF_MASK & 8) k3_gateup<<<108, 256, 0, stream>>>(keys, hbuf, stats, wgF, wuF, gu, selkeys, selh, sell2);
    if (PROF_MASK & 16) k4_down<<<80, 512, 0, stream>>>(gu, wdF, selkeys, selh, sell2, out);
}
```

```cpp
#include <hip/hip_runtime.h>
#include <hip/hip_bf16.h>

typedef unsigned short u16;
typedef unsigned int u32;
typedef __attribute__((ext_vector_type(8))) short bf16x8;
typedef __attribute__((ext_vector_type(4))) short s16x4;
typedef __attribute__((ext_vector_type(4))) float f32x4;
typedef __attribute__((ext_vector_type(2))) float f32x2;
typedef __attribute__((ext_vector_type(2))) __bf16 bfx2;
typedef __attribute__((ext_vector_type(4))) unsigned u32x4;
typedef __attribute__((ext_vector_type(2))) unsigned u32x2;

constexpr int S = 2048, D = 640, F = 1728, NSEL = 8;

#define STAMP(kid, i)
#define STAMPN(kid, i)

__device__ __forceinline__ u32 pk2(float a, float b) {
    f32x2 v = {a, b};
    bfx2 r = __builtin_convertvector(v, bfx2);
    return __builtin_bit_cast(u32, r);
}
__device__ __forceinline__ float lo2f(u32 p) { return __uint_as_float(p << 16); }
__device__ __forceinline__ float hi2f(u32 p) { return __uint_as_float(p & 0xffff0000u); }
__device__ __forceinline__ float rbf(float a) { return lo2f(pk2(a, 0.f)); }

template <int CTRL, int RM> __device__ __forceinline__ int dpp_i(int v) {
    return __builtin_amdgcn_update_dpp(0, v, CTRL, RM, 0xf, true);
}
__device__ __forceinline__ float wave_sum(float v) {
    v += __int_as_float(dpp_i<0x111, 0xf>(__float_as_int(v)));
    v += __int_as_float(dpp_i<0x112, 0xf>(__float_as_int(v)));
    v += __int_as_float(dpp_i<0x114, 0xf>(__float_as_int(v)));
    v += __int_as_float(dpp_i<0x118, 0xf>(__float_as_int(v)));
    v += __int_as_float(dpp_i<0x142, 0xa>(__float_as_int(v)));
    v += __int_as_float(dpp_i<0x143, 0xc>(__float_as_int(v)));
    return __int_as_float(__builtin_amdgcn_readlane(__float_as_int(v), 63));
}
__device__ __forceinline__ u32 umax_(u32 a, u32 b) { return a > b ? a : b; }
__device__ __forceinline__ u32 wave_max_u32(u32 v) {
    v = umax_(v, (u32)dpp_i<0x111, 0xf>((int)v));
    v = umax_(v, (u32)dpp_i<0x112, 0xf>((int)v));
    v = umax_(v, (u32)dpp_i<0x114, 0xf>((int)v));
    v = umax_(v, (u32)dpp_i<0x118, 0xf>((int)v));
    v = umax_(v, (u32)dpp_i<0x142, 0xa>((int)v));
    v = umax_(v, (u32)dpp_i<0x143, 0xc>((int)v));
    return (u32)__builtin_amdgcn_readlane((int)v, 63);
}
#define KEEP4(val_) asm volatile("" ::"v"((val_).x), "v"((val_).y), "v"((val_).z), "v"((val_).w))

typedef __attribute__((address_space(3))) s16x4* lds_s16x4_ptr;
__device__ __forceinline__ s16x4 tr_read(const u16* p) {
    return __builtin_amdgcn_ds_read_tr16_b64_v4i16((lds_s16x4_ptr)p);
}
__device__ __forceinline__ u32 lds_addr(const u16* p) {
    return (u32)(size_t)(const __attribute__((address_space(3))) u16*)p;
}
__device__ __forceinline__ void st16(void* p, u32x4 v) {
    *(u32x4*)p = v;
}
__device__ __forceinline__ void wave_lds_fence() {
    __builtin_amdgcn_fence(__ATOMIC_RELEASE, "wavefront");
    __builtin_amdgcn_wave_barrier();
    __builtin_amdgcn_fence(__ATOMIC_ACQUIRE, "wavefront");
}

__global__ __launch_bounds__(640) void k0_kv(const u16* __restrict__ k, const u16* __restrict__ v,
                                             const u16* __restrict__ shift_k, u16* __restrict__ uT,
                                             const u16* __restrict__ pf0, const u16* __restrict__ pf1,
                                             const u16* __restrict__ pf2, const u16* __restrict__ pf3,
                                             const u16* __restrict__ pf4) {
    __shared__ __attribute__((aligned(16))) u16 tile[80 * 72];
    const int b = blockIdx.x;
    const int ct = b & 7, rt = b >> 3;
    const int tid = threadIdx.x;
    const int rg = tid / 40, cp = tid - rg * 40;
    const int t0 = rt * 64 + rg * 4;
    const int c = ct * 40 + cp;
    STAMP(0, 0);
    u32 kk[7], vv[4];
#pragma unroll
    for (int i = 0; i < 7; ++i) {
        int t = t0 - 3 + i;
        kk[i] = (t >= 0) ? *(const u32*)(k + (size_t)t * D + 2 * c) : 0u;
    }
#pragma unroll
    for (int i = 0; i < 4; ++i) vv[i] = *(const u32*)(v + (size_t)(t0 + i) * D + 2 * c);
    const u32x4 sw = *(const u32x4*)(shift_k + 8 * c);
    const u32x4 pa = *(const u32x4*)(pf0 + (size_t)(b * 640 + tid) * 8);
    const u32x4 pb = *(const u32x4*)(pf1 + (size_t)(b * 640 + tid) * 8);
    const float a0 = lo2f(sw.x), a1 = hi2f(sw.x), a2 = lo2f(sw.y), a3 = hi2f(sw.y);
    const float b0 = lo2f(sw.z), b1 = hi2f(sw.z), b2 = lo2f(sw.w), b3 = hi2f(sw.w);
    float ua[4], ub[4];
#pragma unroll
    for (int i = 0; i < 4; ++i) {
        u32 p0 = pk2(lo2f(kk[i]) * a3, hi2f(kk[i]) * b3);
        u32 p1 = pk2(lo2f(kk[i + 1]) * a2, hi2f(kk[i + 1]) * b2);
        u32 p2 = pk2(lo2f(kk[i + 2]) * a1, hi2f(kk[i + 2]) * b1);
        u32 p3 = pk2(lo2f(kk[i + 3]) * a0, hi2f(kk[i + 3]) * b0);
        u32 s = pk2(lo2f(p0) + lo2f(p1), hi2f(p0) + hi2f(p1));
        s = pk2(lo2f(s) + lo2f(p2), hi2f(s) + hi2f(p2));
        s = pk2(lo2f(s) + lo2f(p3), hi2f(s) + hi2f(p3));
        ua[i] = lo2f(s) * lo2f(vv[i]);
        ub[i] = hi2f(s) * hi2f(vv[i]);
    }
    u32x2 oa, ob;
    oa.x = pk2(ua[3], ua[2]); oa.y = pk2(ua[1], ua[0]);
    ob.x = pk2(ub[3], ub[2]); ob.y = pk2(ub[1], ub[0]);
    *(u32x2*)(tile + (2 * cp) * 72 + ((rg * 4) ^ 4)) = oa;
    *(u32x2*)(tile + (2 * cp + 1) * 72 + ((rg * 4) ^ 4)) = ob;
    STAMPN(0, 1);
    __syncthreads();
    {
        const int ch = tid >> 3, pc = tid & 7;
        const u32x4 o = *(const u32x4*)(tile + ch * 72 + pc * 8);
        st16(uT + (size_t)(ct * 80 + ch) * S + rt * 64 + pc * 8, o);
    }
    KEEP4(pa);
    KEEP4(pb);
    STAMP(0, 2);
}

constexpr int UPAD = 256;
constexpr int KCS = 2080;
constexpr int WCH = 138240;

#define TILE_POS 1
constexpr int K1_CONV_WGS = D;
#define K1_EXTRA 0
__global__ __launch_bounds__(256, 3) void k1_conv(const u16* __restrict__ uT, const u16* __restrict__ ssm_k,
                                               const u16* __restrict__ ssm_D, u16* __restrict__ y2T,
                                               const u16* __restrict__ w_gate, const u16* __restrict__ w_up,
                                               const u16* __restrict__ w_down, u16* __restrict__ wgF,
                                               u16* __restrict__ wuF, u16* __restrict__ wdF) {
    __shared__ __attribute__((aligned(16))) u16 lds[UPAD + S + 12288 + 3 * 2048];
    u16* uL = lds;
    u16* kL = lds + UPAD + S;
    f32x4* part = (f32x4*)(lds + UPAD + S);
    const int tid = threadIdx.x;
    const int w = __builtin_amdgcn_readfirstlane(tid >> 6), lane = tid & 63;
    const int d = blockIdx.x;
    const u16* urow = uT + (size_t)d * S;
    const u16* krow = ssm_k + (size_t)d * S;
    STAMP(1, 0);
    const u32x4 z4 = {0u, 0u, 0u, 0u};
    u32x4 ur, kd;
    u32x2 ke;
    const int T = w * D + d;
    const int tm = T / 540, trem = T - tm * 540;
    const int tks = (tm < 2) ? trem / 27 : trem / 10;
    const int tcg = (tm < 2) ? trem - tks * 27 : trem - tks * 10;
    u16* tl = lds + UPAD + S + 12288 + w * 2048;
    ur = *(const u32x4*)(urow + tid * 8);
    kd = *(const u32x4*)(krow + tid * 8);
    ke = *(const u32x2*)(krow + (tid < 255 ? tid * 8 + 8 : 0));
    const u32 dpair = ((const u32*)ssm_D)[d >> 1];
    const float Dv = (d & 1) ? hi2f(dpair) : lo2f(dpair);
    if (tid == 255) ke = (u32x2){0u, 0u};
    *(u32x4*)(uL + UPAD + tid * 8) = ur;
    if (tid < 32) *(u32x4*)(uL + tid * 8) = z4;
    {
        u32 d0 = kd.x, d1 = kd.y, d2 = kd.z, d3 = kd.w, d4 = ke.x, d5 = ke.y;
        u32 s0 = __builtin_amdgcn_alignbit(d1, d0, 16);
        u32 s1 = __builtin_amdgcn_alignbit(d2, d1, 16);
        u32 s2 = __builtin_amdgcn_alignbit(d3, d2, 16);
        u32 s3 = __builtin_amdgcn_alignbit(d4, d3, 16);
        u32 s4 = __builtin_amdgcn_alignbit(d5, d4, 16);
        u32x4 c0 = {d0, d1, d2, d3}, c1 = {s0, s1, s2, s3}, c2 = {d1, d2, d3, d4}, c3 = {s1, s2, s3, s4};
        const int Y = tid + 2;
        *(u32x4*)(kL + 0 * KCS + Y * 8) = c0;
        *(u32x4*)(kL + 1 * KCS + Y * 8) = c1;
        *(u32x4*)(kL + 2 * KCS + Y * 8) = c2;
        *(u32x4*)(kL + 3 * KCS + Y * 8) = c3;
        if (tid == 0) {
            u32x4 e1 = {0u, 0u, 0u, d0 << 16}, e2 = {0u, 0u, 0u, d0}, e3 = {0u, 0u, d0 << 16, s0};
            *(u32x4*)(kL + 0 * KCS + 0) = z4; *(u32x4*)(kL + 0 * KCS + 8) = z4;
            *(u32x4*)(kL + 1 * KCS + 0) = z4; *(u32x4*)(kL + 1 * KCS + 8) = e1;
            *(u32x4*)(kL + 2 * KCS + 0) = z4; *(u32x4*)(kL + 2 * KCS + 8) = e2;
            *(u32x4*)(kL + 3 * KCS + 0) = z4; *(u32x4*)(kL + 3 * KCS + 8) = e3;
        }
    }
    __syncthreads();
    STAMPN(1, 1);
    if (T < 1620) {
        const u16* src = (tm < 2) ? ((tm ? w_up : w_gate) + (size_t)(tks * 32) * F + tcg * 64)
                                  : (w_down + (size_t)(tks * 32) * D + tcg * 64);
        const int ldm = (tm < 2) ? F : D;
        const u16* g0 = src + (size_t)(lane >> 3) * ldm + (lane & 7) * 8;
        const u16* g1 = g0 + (size_t)8 * ldm;
        const u16* g2 = g0 + (size_t)16 * ldm;
        const u16* g3 = g0 + (size_t)24 * ldm;
        const u32 l0 = __builtin_amdgcn_readfirstlane(lds_addr(tl));
        u32 keep;
        asm volatile(
            "s_mov_b32 %0, m0\n\t"
            "s_mov_b32 m0, %5\n\ts_nop 0\n\tglobal_load_lds_dwordx4 %1, off\n\t"
            "s_add_u32 m0, %5, 0x400\n\ts_nop 0\n\tglobal_load_lds_dwordx4 %2, off\n\t"
            "s_add_u32 m0, %5, 0x800\n\ts_nop 0\n\tglobal_load_lds_dwordx4 %3, off\n\t"
            "s_add_u32 m0, %5, 0xc00\n\ts_nop 0\n\tglobal_load_lds_dwordx4 %4, off\n\t"
            "s_mov_b32 m0, %0"
            : "=&s"(keep)
            : "v"(g0), "v"(g1), "v"(g2), "v"(g3), "s"(l0)
            : "memory", "scc");
    }
    const int li = lane & 15, g = lane >> 4;
    const u16* aBase = kL + ((li + 1) & 3) * KCS + ((li + 1) & ~3) + 8 * g + 64 * w;
    const u16* bBase = uL + UPAD + 16 * li + 8 - 8 * g - 64 * w;
    f32x4 acc[8];
#pragma unroll
    for (int c = 0; c < 8; ++c) acc[c] = (f32x4){0.f, 0.f, 0.f, 0.f};
    const u32 aAddr = lds_addr(aBase);
    const u32 bAddr = lds_addr(bBase - 32);
    s16x4 alo[2][8], ahi[2][8];
    bf16x8 bf[2][8];
#define LOAD_FRAGS(r)                                                                                   \
    _Pragma("unroll") for (int n = 0; n < 8; ++n) {                                                     \
        asm volatile("ds_read_b64 %0, %1 offset:%2" : "=v"(alo[r][n]) : "v"(aAddr), "i"(64 * (8 * n + r))); \
        asm volatile("ds_read_b64 %0, %1 offset:%2" : "=v"(ahi[r][n]) : "v"(aAddr), "i"(64 * (8 * n + r) + 8)); \
        asm volatile("ds_read_b128 %0, %1 offset:%2" : "=v"(bf[r][n]) : "v"(bAddr), "i"(64 * (8 * n - r) + 64)); \
    }
#define MFMA_ALL(r)                                                                                     \
    _Pragma("unroll") for (int sum = 0; sum < 8; ++sum) _Pragma("unroll") for (int n = 0; n <= sum; ++n) \
        acc[sum] = __builtin_amdgcn_mfma_f32_16x16x32_bf16(                                             \
            __builtin_shufflevector(alo[r][n], ahi[r][n], 0, 1, 2, 3, 4, 5, 6, 7), bf[r][sum - n], acc[sum], 0, 0, 0);
    LOAD_FRAGS(0)
    asm volatile("s_waitcnt lgkmcnt(0)" ::: "memory");
    __builtin_amdgcn_sched_barrier(0);
    LOAD_FRAGS(1)
    __builtin_amdgcn_sched_barrier(0);
    MFMA_ALL(0)
    __builtin_amdgcn_sched_barrier(0);
    asm volatile("s_waitcnt lgkmcnt(0)" ::: "memory");
    __builtin_amdgcn_sched_barrier(0);
    MFMA_ALL(1)
#undef LOAD_FRAGS
#undef MFMA_ALL
    STAMPN(1, 2);
    __syncthreads();
#pragma unroll
    for (int c = 0; c < 8; ++c) {
        const int o = c >> 1;
        if (o != w) part[((o * 3 + ((w - o - 1) & 3)) * 2 + (c & 1)) * 64 + lane] = acc[c];
    }
    __syncthreads();
    STAMPN(1, 3);
    if (T < 1620) {
        asm volatile("s_waitcnt vmcnt(0)" ::: "memory");
#pragma unroll
        for (int j = 0; j < 4; ++j) {
            const u16* rp = tl + (8 * g + (li >> 2)) * 64 + 16 * j + 4 * (li & 3);
            s16x4 b0 = tr_read(rp), b1 = tr_read(rp + 4 * 64);
            bf16x8 fr = __builtin_shufflevector(b0, b1, 0, 1, 2, 3, 4, 5, 6, 7);
            u16* dst;
            if (tm < 2) dst = (tm ? wuF : wgF) + ((size_t)((tcg * 4 + j) * 20 + tks) * 64 + lane) * 8;
            else dst = wdF + ((size_t)((tcg * 8 + 2 * j + (li >> 3)) * 54 + tks) * 32 + g * 8 + (li & 7)) * 8;
            asm volatile("global_store_dwordx4 %0, %1, off sc1\n\ts_nop 1" ::"v"(dst), "v"(fr) : "memory");
        }
    }
    u16* yrow = y2T + (size_t)d * S;
    u32x2 ov[2];
#pragma unroll
    for (int j = 0; j < 2; ++j) {
        const int c = 2 * w + j;
        f32x4 own = acc[j];
#pragma unroll
        for (int q = 1; q < 4; ++q) own = (w == q) ? acc[2 * q + j] : own;
        f32x4 a0 = part[((w * 3 + 0) * 2 + j) * 64 + lane], a1 = part[((w * 3 + 1) * 2 + j) * 64 + lane];
        f32x4 a2 = part[((w * 3 + 2) * 2 + j) * 64 + lane];
        f32x4 a = (a0 + a1) + (a2 + own);
        const int tb = 256 * c + 16 * li + 8 * (g >> 1);
        u32x2 uu = *(const u32x2*)(uL + UPAD + tb + 4 * (1 - (g & 1)));
        float u3 = lo2f(uu.x), u2 = hi2f(uu.x), u1 = lo2f(uu.y), u0 = hi2f(uu.y);
        u32 y01 = pk2(a[0], a[1]);
        u32 y23 = pk2(a[2], a[3]);
        u32 w01 = pk2(u0 * Dv, u1 * Dv);
        u32 w23 = pk2(u2 * Dv, u3 * Dv);
        ov[j].x = pk2(lo2f(y01) + lo2f(w01), hi2f(y01) + hi2f(w01));
        ov[j].y = pk2(lo2f(y23) + lo2f(w23), hi2f(y23) + hi2f(w23));
    }
    {
        auto rx = __builtin_amdgcn_permlane16_swap(ov[0].x, ov[1].x, false, false);
        auto ry = __builtin_amdgcn_permlane16_swap(ov[0].y, ov[1].y, false, false);
        u32x4 o = {rx[0], ry[0], rx[1], ry[1]};
        st16(yrow + 256 * (2 * w + (g & 1)) + 16 * li + 8 * (g >> 1), o);
    }
    STAMPN(1, 4);
    STAMP(1, 5);
}

__device__ __forceinline__ void unpack10(const u32x4& a, u32 b, float* f) {
    f[0] = lo2f(a.x); f[1] = hi2f(a.x); f[2] = lo2f(a.y); f[3] = hi2f(a.y);
    f[4] = lo2f(a.z); f[5] = hi2f(a.z); f[6] = lo2f(a.w); f[7] = hi2f(a.w);
    f[8] = lo2f(b); f[9] = hi2f(b);
}
__device__ __forceinline__ void ln_stats(const float* x, float& m_bf, float& s_bf) {
    float s = ((x[0] + x[1]) + (x[2] + x[3])) + ((x[4] + x[5]) + (x[6] + x[7])) + (x[8] + x[9]);
    s = wave_sum(s);
    const float mean = s * (1.0f / D);
    float q0 = 0.f, q1 = 0.f;
#pragma unroll
    for (int i = 0; i < 10; i += 2) {
        float d0 = x[i] - mean, d1 = x[i + 1] - mean;
        q0 += d0 * d0;
        q1 += d1 * d1;
    }
    float q = wave_sum(q0 + q1);
    const float var = q * (1.0f / D);
    m_bf = rbf(mean);
    float vb = rbf(var);
    float ve = rbf(vb + 1e-5f);
    s_bf = rbf(sqrtf(ve));
}

__global__ __launch_bounds__(512) void k2_row(const u16* __restrict__ q, const u16* __restrict__ y2T,
                                              const u16* __restrict__ w_r, u16* __restrict__ out,
                                              u16* __restrict__ hbuf, float* __restrict__ stats,
                                              u32* __restrict__ keys) {
    __shared__ __attribute__((aligned(16))) u32 yt[8][320];
    const int b = blockIdx.x;
    const int tb = (b & 7) * 32 + (b >> 3);
    const int t0 = tb * 8;
    const int tid = threadIdx.x;
    const int w = tid >> 6, lane = tid & 63;
    const int t = t0 + w;
    STAMP(2, 0);
    const u32x4 xa = *(const u32x4*)(q + (size_t)t * D + 8 * lane);
    const u32 xb = *(const u32*)(q + (size_t)t * D + 512 + 2 * lane);
    u32x4 ya = {0u, 0u, 0u, 0u}, yb = {0u, 0u, 0u, 0u};
    if (tid < 320) {
        ya = *(const u32x4*)(y2T + (size_t)(2 * tid) * S + t0);
        yb = *(const u32x4*)(y2T + (size_t)(2 * tid + 1) * S + t0);
    }
    const u32x4 wa = *(const u32x4*)(w_r + 8 * lane);
    const u32 wb = *(const u32*)(w_r + 512 + 2 * lane);
    float x[10];
    unpack10(xa, xb, x);
    float m1, s1;
    ln_stats(x, m1, s1);
    const float r1 = 1.0f / s1;
    STAMP(2, 1);
    if (tid < 320) {
        yt[0][tid] = __builtin_amdgcn_perm(yb.x, ya.x, 0x05040100);
        yt[1][tid] = __builtin_amdgcn_perm(yb.x, ya.x, 0x07060302);
        yt[2][tid] = __builtin_amdgcn_perm(yb.y, ya.y, 0x05040100);
        yt[3][tid] = __builtin_amdgcn_perm(yb.y, ya.y, 0x07060302);
        yt[4][tid] = __builtin_amdgcn_perm(yb.z, ya.z, 0x05040100);
        yt[5][tid] = __builtin_amdgcn_perm(yb.z, ya.z, 0x07060302);
        yt[6][tid] = __builtin_amdgcn_perm(yb.w, ya.w, 0x05040100);
        yt[7][tid] = __builtin_amdgcn_perm(yb.w, ya.w, 0x07060302);
    }
    u32 l1p[5];
#pragma unroll
    for (int i = 0; i < 10; i += 2) {
        u32 dd = pk2(x[i] - m1, x[i + 1] - m1);
        l1p[i >> 1] = pk2(lo2f(dd) * r1, hi2f(dd) * r1);
    }
    __syncthreads();
    STAMP(2, 2);
    const u32x4 y4 = *(const u32x4*)(&yt[w][4 * lane]);
    const u32 y1 = yt[w][256 + lane];
    float y[10];
    unpack10(y4, y1, y);
    float h[10];
    u32 hp[5], op[5];
#pragma unroll
    for (int i = 0; i < 10; i += 2) {
        u32 l1 = l1p[i >> 1];
        u32 a = pk2(lo2f(l1) * y[i], hi2f(l1) * y[i + 1]);
        u32 hh = pk2(x[i] + lo2f(a), x[i + 1] + hi2f(a));
        hp[i >> 1] = hh;
        h[i] = lo2f(hh);
        h[i + 1] = hi2f(hh);
    }
    st16(hbuf + (size_t)t * D + 8 * lane, (u32x4){hp[0], hp[1], hp[2], hp[3]});
    {
        u32x4 tq = {(u32)__builtin_amdgcn_mov_dpp((int)hp[4], 0x00, 0xf, 0xf, true), (u32)__builtin_amdgcn_mov_dpp((int)hp[4], 0x55, 0xf, 0xf, true),
                    (u32)__builtin_amdgcn_mov_dpp((int)hp[4], 0xaa, 0xf, 0xf, true), (u32)__builtin_amdgcn_mov_dpp((int)hp[4], 0xff, 0xf, 0xf, true)};
        if ((lane & 3) == 0) st16(hbuf + (size_t)t * D + 512 + 2 * lane, tq);
    }
    float m2, s2;
    ln_stats(h, m2, s2);
    const float r2 = 1.0f / s2;
    float wr[10];
    unpack10(wa, wb, wr);
    float racc0 = 0.f, racc1 = 0.f;
#pragma unroll
    for (int i = 0; i < 10; i += 2) {
        u32 dd = pk2(h[i] - m2, h[i + 1] - m2);
        u32 l2 = pk2(lo2f(dd) * r2, hi2f(dd) * r2);
        racc0 += lo2f(l2) * wr[i];
        racc1 += hi2f(l2) * wr[i + 1];
        op[i >> 1] = pk2(h[i] + lo2f(l2), h[i + 1] + hi2f(l2));
    }
    st16(out + (size_t)t * D + 8 * lane, (u32x4){op[0], op[1], op[2], op[3]});
    {
        u32x4 tq = {(u32)__builtin_amdgcn_mov_dpp((int)op[4], 0x00, 0xf, 0xf, true), (u32)__builtin_amdgcn_mov_dpp((int)op[4], 0x55, 0xf, 0xf, true),
                    (u32)__builtin_amdgcn_mov_dpp((int)op[4], 0xaa, 0xf, 0xf, true), (u32)__builtin_amdgcn_mov_dpp((int)op[4], 0xff, 0xf, 0xf, true)};
        if ((lane & 3) == 0) st16(out + (size_t)t * D + 512 + 2 * lane, tq);
    }
    const float racc = wave_sum(racc0 + racc1);
    STAMP(2, 3);
    if (lane == 0) {
        u32 rb = pk2(racc, 0.f) & 0xffffu;
        u32 ord = (rb & 0x8000u) ? (rb ^ 0xffffu) : (rb ^ 0x8000u);
        keys[t] = (ord << 16) | (u32)(0xffff - t);
        *(f32x2*)(stats + 2 * t) = (f32x2){m2, r2};
    }
    STAMP(2, 4);
}

__device__ __forceinline__ float key_r(u32 key) {
    u32 ord = key >> 16;
    u32 rb = (ord & 0x8000u) ? (ord ^ 0x8000u) : (ord ^ 0xffffu);
    return __uint_as_float(rb << 16);
}

constexpr int XS = 648;
__global__ __launch_bounds__(512) void k3_gateup(const u32* __restrict__ keys, const u16* __restrict__ hbuf,
                                                 const float* __restrict__ stats, const u16* __restrict__ wgF,
                                                 const u16* __restrict__ wuF, u16* __restrict__ gu,
                                                 u32* __restrict__ selkeys, u16* __restrict__ selh,
                                                 u16* __restrict__ sell2) {
    __shared__ __attribute__((aligned(16))) u16 xs[8 * XS];
    __shared__ __attribute__((aligned(16))) float red[8][2][8][16];
    __shared__ u32 cand[64];
    const int b = blockIdx.x;
    const int slice = b;
    const int f0 = slice * 16;
    const int tid = threadIdx.x, lane = tid & 63;
    const int w = __builtin_amdgcn_readfirstlane(tid >> 6);
    STAMP(3, 0);
    u32 a[4];
    {
        u32x4 k0 = *(const u32x4*)(keys + tid * 4);
        a[0] = k0.x; a[1] = k0.y; a[2] = k0.z; a[3] = k0.w;
    }
    bf16x8 bg[3], bu[3];
#pragma unroll
    for (int i = 0; i < 3; ++i) {
        const int ks = (w + 8 * i < 20) ? (w + 8 * i) : w;
        bg[i] = *(const bf16x8*)(wgF + ((size_t)(slice * 20 + ks) * 64 + lane) * 8);
        bu[i] = *(const bf16x8*)(wuF + ((size_t)(slice * 20 + ks) * 64 + lane) * 8);
    }
#define CE(i, j) { u32 hi_ = a[i] > a[j] ? a[i] : a[j]; u32 lo_ = a[i] > a[j] ? a[j] : a[i]; a[i] = hi_; a[j] = lo_; }
    CE(0, 1) CE(2, 3) CE(0, 2) CE(1, 3) CE(1, 2)
#undef CE
    u32 mine = 0;
#pragma unroll
    for (int rd = 0; rd < 8; ++rd) {
        u32 m = wave_max_u32(a[0]);
        bool win = (a[0] == m);
        a[0] = win ? a[1] : a[0];
        a[1] = win ? a[2] : a[1];
        a[2] = win ? a[3] : a[2];
        a[3] = win ? 0u : a[3];
        if (lane == rd) mine = m;
    }
    if (lane < 8) cand[w * 8 + lane] = mine;
    STAMPN(3, 1);
    __syncthreads();
    STAMPN(3, 2);
    {
        u32 c = cand[lane];
        u32 key = 0;
        for (int rd = 0; rd <= w; ++rd) {
            key = wave_max_u32(c);
            c = (c == key) ? 0u : c;
        }
        const int ta = 0xffff - (int)(key & 0xffffu);
        const u32x4 ha = *(const u32x4*)(hbuf + (size_t)ta * D + 8 * lane);
        const u32 hb = *(const u32*)(hbuf + (size_t)ta * D + 512 + 2 * lane);
        const f32x2 st0 = *(const f32x2*)(stats + 2 * ta);
        STAMP(3, 3);
        const float m2 = st0.x, r2 = st0.y;
        float h[10];
        unpack10(ha, hb, h);
        u32 l2[5];
#pragma unroll
        for (int i = 0; i < 10; i += 2) {
            u32 dd = pk2(h[i] - m2, h[i + 1] - m2);
            l2[i >> 1] = pk2(lo2f(dd) * r2, hi2f(dd) * r2);
        }
        *(u32x4*)(xs + w * XS + 8 * lane) = (u32x4){l2[0], l2[1], l2[2], l2[3]};
        *(u32*)(xs + w * XS + 512 + 2 * lane) = l2[4];
        if (b == 0) {
            *(u32x4*)(selh + w * D + 8 * lane) = ha;
            *(u32*)(selh + w * D + 512 + 2 * lane) = hb;
            *(u32x4*)(sell2 + w * D + 8 * lane) = (u32x4){l2[0], l2[1], l2[2], l2[3]};
            *(u32*)(sell2 + w * D + 512 + 2 * lane) = l2[4];
            if (lane == 0) selkeys[w] = key;
        }
    }
    STAMPN(3, 4);
    __syncthreads();
    STAMPN(3, 5);
    {
        const int li = lane & 15, g = lane >> 4;
        f32x4 ag = {0.f, 0.f, 0.f, 0.f}, au = {0.f, 0.f, 0.f, 0.f};
#pragma unroll
        for (int s = 0; s < 3; ++s) {
            if (w + 8 * s < 20) {
                const int k0 = (w + 8 * s) * 32;
                bf16x8 af = *(const bf16x8*)(xs + (li & 7) * XS + k0 + 8 * g);
                ag = __builtin_amdgcn_mfma_f32_16x16x32_bf16(af, bg[s], ag, 0, 0, 0);
                au = __builtin_amdgcn_mfma_f32_16x16x32_bf16(af, bu[s], au, 0, 0, 0);
            }
        }
        if (g < 2) {
#pragma unroll
            for (int rr = 0; rr < 4; ++rr) {
                red[w][0][4 * g + rr][li] = ag[rr];
                red[w][1][4 * g + rr][li] = au[rr];
            }
        }
    }
    STAMPN(3, 6);
    __syncthreads();
    if (tid < 128) {
        const int tok = tid >> 4, col = tid & 15;
        float gs = ((red[0][0][tok][col] + red[1][0][tok][col]) + (red[2][0][tok][col] + red[3][0][tok][col])) +
                   ((red[4][0][tok][col] + red[5][0][tok][col]) + (red[6][0][tok][col] + red[7][0][tok][col]));
        float us = ((red[0][1][tok][col] + red[1][1][tok][col]) + (red[2][1][tok][col] + red[3][1][tok][col])) +
                   ((red[4][1][tok][col] + red[5][1][tok][col]) + (red[6][1][tok][col] + red[7][1][tok][col]));
        float gb = rbf(gs), ub = rbf(us);
        float sg = rbf(gb / (1.0f + __expf(-gb)));
        u32 o = pk2(sg * ub, 0.f);
        const int f = f0 + col;
        gu[((f >> 5) * 4 + ((f >> 3) & 3)) * 64 + tok * 8 + (f & 7)] = (u16)(o & 0xffffu);
    }
    STAMP(3, 7);
}

__global__ __launch_bounds__(512) void k4_down(const u16* __restrict__ gu, const u16* __restrict__ wdF,
                                               const u32* __restrict__ selkeys, const u16* __restrict__ selh,
                                               const u16* __restrict__ sell2, u16* __restrict__ out) {
    __shared__ __attribute__((aligned(16))) float red[8][8][16];
    const int slice = blockIdx.x;
    const int d0 = slice * 8;
    const int tid = threadIdx.x, w = tid >> 6, lane = tid & 63;
    const int li = lane & 15, g = lane >> 4;
    STAMP(4, 0);
    bf16x8 af[7], bf[7];
#pragma unroll
    for (int j = 0; j < 7; ++j) {
        int s = w + 8 * j;
        if (s < 54) {
            af[j] = *(const bf16x8*)(gu + (s * 4 + g) * 64 + (li & 7) * 8);
            bf[j] = *(const bf16x8*)(wdF + ((size_t)(slice * 54 + s) * 32 + g * 8 + (li & 7)) * 8);
        }
    }
    float hv = 0.f, l2v = 0.f;
    u32 key = 0;
    if (tid < 64) {
        const int tok = tid >> 3, col = tid & 7;
        key = selkeys[tok];
        hv = __uint_as_float(((u32)selh[tok * D + d0 + col]) << 16);
        l2v = __uint_as_float(((u32)sell2[tok * D + d0 + col]) << 16);
    }
    STAMP(4, 1);
    {
        f32x4 acc = {0.f, 0.f, 0.f, 0.f};
#pragma unroll
        for (int j = 0; j < 7; ++j) {
            int s = w + 8 * j;
            if (s < 54) acc = __builtin_amdgcn_mfma_f32_16x16x32_bf16(af[j], bf[j], acc, 0, 0, 0);
        }
        if (g < 2) {
#pragma unroll
            for (int rr = 0; rr < 4; ++rr) red[w][4 * g + rr][li] = acc[rr];
        }
    }
    STAMP(4, 2);
    __syncthreads();
    if (tid < 64) {
        const int tok = tid >> 3, col = tid & 7;
        const int trow = 0xffff - (int)(key & 0xffffu);
        const float rv = key_r(key);
        const float rs = rbf(1.0f / (1.0f + __expf(-rv)));
        float s = ((red[0][tok][col] + red[1][tok][col]) + (red[2][tok][col] + red[3][tok][col])) +
                  ((red[4][tok][col] + red[5][tok][col]) + (red[6][tok][col] + red[7][tok][col]));
        float y = rbf(s);
        y = rbf(y * rs);
        float o1 = rbf(l2v + y);
        u32 o = pk2(hv + o1, 0.f);
        out[(size_t)trow * D + d0 + col] = (u16)(o & 0xffffu);
    }
    STAMP(4, 3);
}

extern "C" void kernel_launch(void* const* d_in, const int* in_sizes, int n_in, void* d_out, int out_size,
                              void* d_ws, size_t ws_size, hipStream_t stream) {
    const u16* q = (const u16*)d_in[0];
    const u16* k = (const u16*)d_in[1];
    const u16* v = (const u16*)d_in[2];
    const u16* shift_k = (const u16*)d_in[3];
    const u16* ssm_k = (const u16*)d_in[4];
    const u16* ssm_D = (const u16*)d_in[5];
    const u16* w_r = (const u16*)d_in[7];
    const u16* w_gate = (const u16*)d_in[8];
    const u16* w_up = (const u16*)d_in[9];
    const u16* w_down = (const u16*)d_in[10];
    u16* out = (u16*)d_out;
    char* ws = (char*)d_ws;
    const size_t TS = (size_t)S * D * 2;
    u16* uT = (u16*)(ws);
    u16* y2T = (u16*)(ws + TS);
    u16* hbuf = (u16*)(ws + 2 * TS);
    char* sm = ws + 3 * TS;
    float* stats = (float*)(sm);
    u32* keys = (u32*)(sm + 16384);
    u32* selkeys = (u32*)(sm + 24576);
    u16* selh = (u16*)(sm + 24576 + 256);
    u16* sell2 = (u16*)(sm + 24576 + 256 + 10240);
    u16* gu = (u16*)(sm + 24576 + 256 + 20480);
    const size_t WS_ = (size_t)D * F * 2;
    u16* wgF = (u16*)(sm + 131072);
    u16* wuF = (u16*)(sm + 131072 + WS_);
    u16* wdF = (u16*)(sm + 131072 + 2 * WS_);
#define PROF_MASK 0xff
    if (PROF_MASK & 1) k0_kv<<<256, 640, 0, stream>>>(k, v, shift_k, uT, ssm_k, q, w_gate, w_up, w_down);
    if (PROF_MASK & 2) k1_conv<<<K1_CONV_WGS + K1_EXTRA, 256, 0, stream>>>(uT, ssm_k, ssm_D, y2T, w_gate, w_up, w_down, wgF, wuF, wdF);
    if (PROF_MASK & 4) k2_row<<<256, 512, 0, stream>>>(q, y2T, w_r, out, hbuf, stats, keys);
    if (PROF_MASK & 8) k3_gateup<<<108, 512, 0, stream>>>(keys, hbuf, stats, wgF, wuF, gu, selkeys, selh, sell2);
    if (PROF_MASK & 16) k4_down<<<80, 512, 0, stream>>>(gu, wdF, selkeys, selh, sell2, out);
}
```

```cpp
#include <hip/hip_runtime.h>
#include <hip/hip_bf16.h>

typedef unsigned short u16;
typedef unsigned int u32;
typedef __attribute__((ext_vector_type(8))) short bf16x8;
typedef __attribute__((ext_vector_type(4))) short s16x4;
typedef __attribute__((ext_vector_type(4))) float f32x4;
typedef __attribute__((ext_vector_type(2))) float f32x2;
typedef __attribute__((ext_vector_type(2))) __bf16 bfx2;
typedef __attribute__((ext_vector_type(4))) unsigned u32x4;
typedef __attribute__((ext_vector_type(2))) unsigned u32x2;

constexpr int S = 2048, D = 640, F = 1728, NSEL = 8;
#define OUT_ST 0

#define STAMP(kid, i)
#define STAMPN(kid, i)

__device__ __forceinline__ u32 pk2(float a, float b) {
    f32x2 v = {a, b};
    bfx2 r = __builtin_convertvector(v, bfx2);
    return __builtin_bit_cast(u32, r);
}
__device__ __forceinline__ float lo2f(u32 p) { return __uint_as_float(p << 16); }
__device__ __forceinline__ float hi2f(u32 p) { return __uint_as_float(p & 0xffff0000u); }
__device__ __forceinline__ float rbf(float a) { return lo2f(pk2(a, 0.f)); }

template <int CTRL, int RM> __device__ __forceinline__ int dpp_i(int v) {
    return __builtin_amdgcn_update_dpp(0, v, CTRL, RM, 0xf, true);
}
__device__ __forceinline__ float wave_sum(float v) {
    v += __int_as_float(dpp_i<0x111, 0xf>(__float_as_int(v)));
    v += __int_as_float(dpp_i<0x112, 0xf>(__float_as_int(v)));
    v += __int_as_float(dpp_i<0x114, 0xf>(__float_as_int(v)));
    v += __int_as_float(dpp_i<0x118, 0xf>(__float_as_int(v)));
    v += __int_as_float(dpp_i<0x142, 0xa>(__float_as_int(v)));
    v += __int_as_float(dpp_i<0x143, 0xc>(__float_as_int(v)));
    return __int_as_float(__builtin_amdgcn_readlane(__float_as_int(v), 63));
}
__device__ __forceinline__ u32 umax_(u32 a, u32 b) { return a > b ? a : b; }
__device__ __forceinline__ u32 wave_max_u32(u32 v) {
    v = umax_(v, (u32)dpp_i<0x111, 0xf>((int)v));
    v = umax_(v, (u32)dpp_i<0x112, 0xf>((int)v));
    v = umax_(v, (u32)dpp_i<0x114, 0xf>((int)v));
    v = umax_(v, (u32)dpp_i<0x118, 0xf>((int)v));
    v = umax_(v, (u32)dpp_i<0x142, 0xa>((int)v));
    v = umax_(v, (u32)dpp_i<0x143, 0xc>((int)v));
    return (u32)__builtin_amdgcn_readlane((int)v, 63);
}
#define KEEP4(val_) asm volatile("" ::"v"((val_).x), "v"((val_).y), "v"((val_).z), "v"((val_).w))

typedef __attribute__((address_space(3))) s16x4* lds_s16x4_ptr;
__device__ __forceinline__ s16x4 tr_read(const u16* p) {
    return __builtin_amdgcn_ds_read_tr16_b64_v4i16((lds_s16x4_ptr)p);
}
__device__ __forceinline__ u32 lds_addr(const u16* p) {
    return (u32)(size_t)(const __attribute__((address_space(3))) u16*)p;
}
__device__ __forceinline__ void st16(void* p, u32x4 v) {
    *(u32x4*)p = v;
}
__device__ __forceinline__ void st16o(void* p, u32x4 v) {
    *(u32x4*)p = v;
}
__device__ __forceinline__ void wave_lds_fence() {
    __builtin_amdgcn_fence(__ATOMIC_RELEASE, "wavefront");
    __builtin_amdgcn_wave_barrier();
    __builtin_amdgcn_fence(__ATOMIC_ACQUIRE, "wavefront");
}

constexpr int UPAD = 256;
constexpr int KCS = 2080;
constexpr int CH_LDS = UPAD + S + 4 * KCS;
constexpr int CONV_WGS = D / 4;
constexpr int SIDE_WGS = 68;

__device__ __forceinline__ u32 shr1(u32 old, u32 v) {
    return (u32)__builtin_amdgcn_update_dpp((int)old, (int)v, 0x138, 0xf, 0xf, false);
}

__global__ __launch_bounds__(512, 2) void k01_conv(const u16* __restrict__ k, const u16* __restrict__ v,
                                                  const u16* __restrict__ shift_k, const u16* __restrict__ ssm_k,
                                                  const u16* __restrict__ ssm_D, u16* __restrict__ y2T,
                                                  const u16* __restrict__ w_gate, const u16* __restrict__ w_up,
                                                  const u16* __restrict__ w_down, u16* __restrict__ wgF,
                                                  u16* __restrict__ wuF, u16* __restrict__ wdF,
                                                  const u16* __restrict__ qpf) {
    __shared__ __attribute__((aligned(16))) u16 lds[4 * CH_LDS];
    const int tid = threadIdx.x;
    const int w = __builtin_amdgcn_readfirstlane(tid >> 6), lane = tid & 63;
    const int li = lane & 15, g = lane >> 4;
    const u32x4 z4 = {0u, 0u, 0u, 0u};
    if (blockIdx.x >= CONV_WGS) {
        const int sb = blockIdx.x - CONV_WGS;
        u16* tl = lds + w * 2048;
        u32x4 ld[3][4];
#pragma unroll
        for (int tt = 0; tt < 3; ++tt) {
            int T = (sb * 8 + w) * 3 + tt;
            T = T < 1620 ? T : 1619;
            const int tm = T / 540, trem = T - tm * 540;
            const int tks = (tm < 2) ? trem / 27 : trem / 10;
            const int tcg = (tm < 2) ? trem - tks * 27 : trem - tks * 10;
            const u16* src = (tm < 2) ? ((tm ? w_up : w_gate) + (size_t)(tks * 32) * F + tcg * 64)
                                      : (w_down + (size_t)(tks * 32) * D + tcg * 64);
            const int ldm = (tm < 2) ? F : D;
#pragma unroll
            for (int i = 0; i < 4; ++i)
                ld[tt][i] = *(const u32x4*)(src + (size_t)(i * 8 + (lane >> 3)) * ldm + (lane & 7) * 8);
        }
        u32x4 pq[5];
#pragma unroll
        for (int i = 0; i < 5; ++i) {
            int ch = (sb * 5 + i) * 512 + tid;
            ch = ch < (S * D / 8) ? ch : 0;
            pq[i] = *(const u32x4*)(qpf + (size_t)ch * 8);
        }
#pragma unroll
        for (int tt = 0; tt < 3; ++tt) {
            const int T = (sb * 8 + w) * 3 + tt;
            wave_lds_fence();
#pragma unroll
            for (int i = 0; i < 4; ++i) *(u32x4*)(tl + (i * 64 + lane) * 8) = ld[tt][i];
            wave_lds_fence();
            if (T < 1620) {
                const int tm = T / 540, trem = T - tm * 540;
                const int tks = (tm < 2) ? trem / 27 : trem / 10;
                const int tcg = (tm < 2) ? trem - tks * 27 : trem - tks * 10;
#pragma unroll
                for (int j = 0; j < 4; ++j) {
                    const u16* rp = tl + (8 * g + (li >> 2)) * 64 + 16 * j + 4 * (li & 3);
                    s16x4 b0 = tr_read(rp), b1 = tr_read(rp + 4 * 64);
                    bf16x8 fr = __builtin_shufflevector(b0, b1, 0, 1, 2, 3, 4, 5, 6, 7);
                    u16* dst;
                    if (tm < 2) dst = (tm ? wuF : wgF) + ((size_t)((tcg * 4 + j) * 20 + tks) * 64 + lane) * 8;
                    else dst = wdF + ((size_t)((tcg * 8 + 2 * j + (li >> 3)) * 54 + tks) * 32 + g * 8 + (li & 7)) * 8;
                    asm volatile("global_store_dwordx4 %0, %1, off sc1\n\ts_nop 1" ::"v"(dst), "v"(fr) : "memory");
                }
            }
        }
#pragma unroll
        for (int i = 0; i < 5; ++i) KEEP4(pq[i]);
        return;
    }
    const int b = blockIdx.x;
    const int grp = (b & 7) * 20 + (b >> 3);
    const int d0 = grp * 4;
    const int t0 = tid * 4;
    STAMP(0, 0);
    u32x2 kk[7], vv[4];
#pragma unroll
    for (int i = 0; i < 4; ++i) kk[3 + i] = *(const u32x2*)(k + (size_t)(t0 + i) * D + d0);
#pragma unroll
    for (int i = 0; i < 4; ++i) vv[i] = *(const u32x2*)(v + (size_t)(t0 + i) * D + d0);
    {
        const int th = (w * 256 - 3) < 0 ? 0 : (w * 256 - 3);
#pragma unroll
        for (int i = 0; i < 3; ++i) kk[i] = *(const u32x2*)(k + (size_t)(th + i) * D + d0);
    }
    const int kb = tid & 255;
    u32x4 kd[2];
    u32x2 ke[2];
#pragma unroll
    for (int j = 0; j < 2; ++j) {
        const u16* krow = ssm_k + (size_t)(d0 + (tid >> 8) + 2 * j) * S;
        kd[j] = *(const u32x4*)(krow + kb * 8);
        ke[j] = *(const u32x2*)(krow + (kb < 255 ? kb * 8 + 8 : 0));
    }
    const u32x4 sw0 = *(const u32x4*)(shift_k + 4 * d0);
    const u32x4 sw1 = *(const u32x4*)(shift_k + 4 * d0 + 8);
    const u32x2 dq = *(const u32x2*)(ssm_D + d0);
    __builtin_amdgcn_sched_barrier(0);
    if (w == 0) { kk[0] = (u32x2){0u, 0u}; kk[1] = (u32x2){0u, 0u}; kk[2] = (u32x2){0u, 0u}; }
#pragma unroll
    for (int i = 0; i < 3; ++i) {
        kk[i].x = shr1(kk[i].x, kk[4 + i].x);
        kk[i].y = shr1(kk[i].y, kk[4 + i].y);
    }
#pragma unroll
    for (int hf = 0; hf < 2; ++hf) {
        const u32x4 sw = hf ? sw1 : sw0;
        const float a0 = lo2f(sw.x), a1 = hi2f(sw.x), a2 = lo2f(sw.y), a3 = hi2f(sw.y);
        const float b0 = lo2f(sw.z), b1 = hi2f(sw.z), b2 = lo2f(sw.w), b3 = hi2f(sw.w);
        float ua[4], ub[4];
#pragma unroll
        for (int i = 0; i < 4; ++i) {
            const u32 x0 = hf ? kk[i].y : kk[i].x, x1 = hf ? kk[i + 1].y : kk[i + 1].x;
            const u32 x2 = hf ? kk[i + 2].y : kk[i + 2].x, x3 = hf ? kk[i + 3].y : kk[i + 3].x;
            const u32 vx = hf ? vv[i].y : vv[i].x;
            u32 p0 = pk2(lo2f(x0) * a3, hi2f(x0) * b3);
            u32 p1 = pk2(lo2f(x1) * a2, hi2f(x1) * b2);
            u32 p2 = pk2(lo2f(x2) * a1, hi2f(x2) * b1);
            u32 p3 = pk2(lo2f(x3) * a0, hi2f(x3) * b0);
            u32 sm = pk2(lo2f(p0) + lo2f(p1), hi2f(p0) + hi2f(p1));
            sm = pk2(lo2f(sm) + lo2f(p2), hi2f(sm) + hi2f(p2));
            sm = pk2(lo2f(sm) + lo2f(p3), hi2f(sm) + hi2f(p3));
            ua[i] = lo2f(sm) * lo2f(vx);
            ub[i] = hi2f(sm) * hi2f(vx);
        }
        u32x2 oa, ob;
        oa.x = pk2(ua[3], ua[2]); oa.y = pk2(ua[1], ua[0]);
        ob.x = pk2(ub[3], ub[2]); ob.y = pk2(ub[1], ub[0]);
        *(u32x2*)(lds + (2 * hf) * CH_LDS + UPAD + (t0 ^ 4)) = oa;
        *(u32x2*)(lds + (2 * hf + 1) * CH_LDS + UPAD + (t0 ^ 4)) = ob;
    }
    if (tid < 128) *(u32x4*)(lds + (tid >> 5) * CH_LDS + (tid & 31) * 8) = z4;
#pragma unroll
    for (int j = 0; j < 2; ++j) {
        u16* kL = lds + ((tid >> 8) + 2 * j) * CH_LDS + UPAD + S;
        u32 e0 = kd[j].x, e1 = kd[j].y, e2 = kd[j].z, e3 = kd[j].w;
        u32 e4 = (kb < 255) ? ke[j].x : 0u, e5 = (kb < 255) ? ke[j].y : 0u;
        u32 s0 = __builtin_amdgcn_alignbit(e1, e0, 16);
        u32 s1 = __builtin_amdgcn_alignbit(e2, e1, 16);
        u32 s2 = __builtin_amdgcn_alignbit(e3, e2, 16);
        u32 s3 = __builtin_amdgcn_alignbit(e4, e3, 16);
        u32 s4 = __builtin_amdgcn_alignbit(e5, e4, 16);
        u32x4 c0 = {e0, e1, e2, e3}, c1 = {s0, s1, s2, s3}, c2 = {e1, e2, e3, e4}, c3 = {s1, s2, s3, s4};
        const int Y = kb + 2;
        *(u32x4*)(kL + 0 * KCS + Y * 8) = c0;
        *(u32x4*)(kL + 1 * KCS + Y * 8) = c1;
        *(u32x4*)(kL + 2 * KCS + Y * 8) = c2;
        *(u32x4*)(kL + 3 * KCS + Y * 8) = c3;
        if (kb == 0) {
            u32x4 f1 = {0u, 0u, 0u, e0 << 16}, f2 = {0u, 0u, 0u, e0}, f3 = {0u, 0u, e0 << 16, s0};
            *(u32x4*)(kL + 0 * KCS + 0) = z4; *(u32x4*)(kL + 0 * KCS + 8) = z4;
            *(u32x4*)(kL + 1 * KCS + 0) = z4; *(u32x4*)(kL + 1 * KCS + 8) = f1;
            *(u32x4*)(kL + 2 * KCS + 0) = z4; *(u32x4*)(kL + 2 * KCS + 8) = f2;
            *(u32x4*)(kL + 3 * KCS + 0) = z4; *(u32x4*)(kL + 3 * KCS + 8) = f3;
        }
    }
    __syncthreads();
    STAMPN(0, 1);
    const int ch = w >> 1, hw = w & 1;
    u16* uL = lds + ch * CH_LDS;
    u16* kL = uL + UPAD + S;
    const u32 aAddr = lds_addr(kL + ((li + 1) & 3) * KCS + ((li + 1) & ~3) + 8 * g + 128 * hw);
    const u32 bAddr = lds_addr(uL + UPAD + 16 * li + 8 - 8 * g - 128 * hw - 96);
    f32x4 acc[8];
#pragma unroll
    for (int c = 0; c < 8; ++c) acc[c] = (f32x4){0.f, 0.f, 0.f, 0.f};
    s16x4 alo[2][8], ahi[2][8];
    bf16x8 bf[2][8];
#define LOAD_FRAGS(buf, r)                                                                                     \
    _Pragma("unroll") for (int n = 0; n < 8; ++n) {                                                            \
        asm volatile("ds_read_b64 %0, %1 offset:%2" : "=v"(alo[buf][n]) : "v"(aAddr), "i"(64 * (8 * n + r)));  \
        asm volatile("ds_read_b64 %0, %1 offset:%2" : "=v"(ahi[buf][n]) : "v"(aAddr), "i"(64 * (8 * n + r) + 8)); \
        asm volatile("ds_read_b128 %0, %1 offset:%2" : "=v"(bf[buf][n]) : "v"(bAddr), "i"(64 * (8 * n - r) + 192)); \
    }
#define MFMA_ALL(buf)                                                                                          \
    _Pragma("unroll") for (int sum = 0; sum < 8; ++sum) _Pragma("unroll") for (int n = 0; n <= sum; ++n)       \
        acc[sum] = __builtin_amdgcn_mfma_f32_16x16x32_bf16(                                                    \
            __builtin_shufflevector(alo[buf][n], ahi[buf][n], 0, 1, 2, 3, 4, 5, 6, 7), bf[buf][sum - n], acc[sum], 0, 0, 0);
#define WAIT_LDS()                                              \
    __builtin_amdgcn_sched_barrier(0);                          \
    asm volatile("s_waitcnt lgkmcnt(0)" ::: "memory");          \
    __builtin_amdgcn_sched_barrier(0);
    LOAD_FRAGS(0, 0)
    WAIT_LDS()
    LOAD_FRAGS(1, 1)
    __builtin_amdgcn_sched_barrier(0);
    MFMA_ALL(0)
    WAIT_LDS()
    LOAD_FRAGS(0, 2)
    __builtin_amdgcn_sched_barrier(0);
    MFMA_ALL(1)
    WAIT_LDS()
    LOAD_FRAGS(1, 3)
    __builtin_amdgcn_sched_barrier(0);
    MFMA_ALL(0)
    WAIT_LDS()
    MFMA_ALL(1)
#undef LOAD_FRAGS
#undef MFMA_ALL
#undef WAIT_LDS
    STAMPN(0, 2);
    __syncthreads();
    f32x4* part = (f32x4*)kL;
    if (hw) {
#pragma unroll
        for (int c = 0; c < 4; ++c) { f32x4 tmp = acc[c]; acc[c] = acc[4 + c]; acc[4 + c] = tmp; }
    }
#pragma unroll
    for (int c = 0; c < 4; ++c) part[((1 - hw) * 4 + c) * 64 + lane] = acc[4 + c];
    __syncthreads();
    STAMPN(0, 3);
    const u32 dsel = (ch & 2) ? dq.y : dq.x;
    const float Dv = (ch & 1) ? hi2f(dsel) : lo2f(dsel);
    u16* yrow = y2T + (size_t)(d0 + ch) * S;
#pragma unroll
    for (int pr = 0; pr < 2; ++pr) {
        u32x2 ov[2];
#pragma unroll
        for (int j = 0; j < 2; ++j) {
            const int cl = 2 * pr + j;
            const int c = 4 * hw + cl;
            f32x4 a = acc[cl] + part[(hw * 4 + cl) * 64 + lane];
            const int tb = 256 * c + 16 * li + 8 * (g >> 1);
            u32x2 uu = *(const u32x2*)(uL + UPAD + tb + 4 * (1 - (g & 1)));
            float u3 = lo2f(uu.x), u2 = hi2f(uu.x), u1 = lo2f(uu.y), u0 = hi2f(uu.y);
            u32 y01 = pk2(a[0], a[1]);
            u32 y23 = pk2(a[2], a[3]);
            u32 w01 = pk2(u0 * Dv, u1 * Dv);
            u32 w23 = pk2(u2 * Dv, u3 * Dv);
            ov[j].x = pk2(lo2f(y01) + lo2f(w01), hi2f(y01) + hi2f(w01));
            ov[j].y = pk2(lo2f(y23) + lo2f(w23), hi2f(y23) + hi2f(w23));
        }
        auto rx = __builtin_amdgcn_permlane16_swap(ov[0].x, ov[1].x, false, false);
        auto ry = __builtin_amdgcn_permlane16_swap(ov[0].y, ov[1].y, false, false);
        u32x4 o = {rx[0], ry[0], rx[1], ry[1]};
        st16(yrow + 256 * (4 * hw + 2 * pr + (g & 1)) + 16 * li + 8 * (g >> 1), o);
    }
    STAMP(0, 4);
}

__device__ __forceinline__ void unpack10(const u32x4& a, u32 b, float* f) {
    f[0] = lo2f(a.x); f[1] = hi2f(a.x); f[2] = lo2f(a.y); f[3] = hi2f(a.y);
    f[4] = lo2f(a.z); f[5] = hi2f(a.z); f[6] = lo2f(a.w); f[7] = hi2f(a.w);
    f[8] = lo2f(b); f[9] = hi2f(b);
}
__device__ __forceinline__ void ln_stats(const float* x, float& m_bf, float& s_bf) {
    float s = ((x[0] + x[1]) + (x[2] + x[3])) + ((x[4] + x[5]) + (x[6] + x[7])) + (x[8] + x[9]);
    s = wave_sum(s);
    const float mean = s * (1.0f / D);
    float q0 = 0.f, q1 = 0.f;
#pragma unroll
    for (int i = 0; i < 10; i += 2) {
        float d0 = x[i] - mean, d1 = x[i + 1] - mean;
        q0 += d0 * d0;
        q1 += d1 * d1;
    }
    float q = wave_sum(q0 + q1);
    const float var = q * (1.0f / D);
    m_bf = rbf(mean);
    float vb = rbf(var);
    float ve = rbf(vb + 1e-5f);
    s_bf = rbf(sqrtf(ve));
}

__global__ __launch_bounds__(512) void k2_row(const u16* __restrict__ q, const u16* __restrict__ y2T,
                                              const u16* __restrict__ w_r, u16* __restrict__ out,
                                              u16* __restrict__ hbuf, float* __restrict__ stats,
                                              u32* __restrict__ keys) {
    __shared__ __attribute__((aligned(16))) u32 yt[8][320];
    const int b = blockIdx.x;
    const int tb = (b & 7) * 32 + (b >> 3);
    const int t0 = tb * 8;
    const int tid = threadIdx.x;
    const int w = tid >> 6, lane = tid & 63;
    const int t = t0 + w;
    STAMP(1, 0);
    const u32x4 xa = *(const u32x4*)(q + (size_t)t * D + 8 * lane);
    const u32 xb = *(const u32*)(q + (size_t)t * D + 512 + 2 * lane);
    u32x4 ya = {0u, 0u, 0u, 0u}, yb = {0u, 0u, 0u, 0u};
    if (tid < 320) {
        ya = *(const u32x4*)(y2T + (size_t)(2 * tid) * S + t0);
        yb = *(const u32x4*)(y2T + (size_t)(2 * tid + 1) * S + t0);
    }
    const u32x4 wa = *(const u32x4*)(w_r + 8 * lane);
    const u32 wb = *(const u32*)(w_r + 512 + 2 * lane);
    float x[10];
    unpack10(xa, xb, x);
    float m1, s1;
    ln_stats(x, m1, s1);
    const float r1 = 1.0f / s1;
    STAMP(1, 1);
    if (tid < 320) {
        yt[0][tid] = __builtin_amdgcn_perm(yb.x, ya.x, 0x05040100);
        yt[1][tid] = __builtin_amdgcn_perm(yb.x, ya.x, 0x07060302);
        yt[2][tid] = __builtin_amdgcn_perm(yb.y, ya.y, 0x05040100);
        yt[3][tid] = __builtin_amdgcn_perm(yb.y, ya.y, 0x07060302);
        yt[4][tid] = __builtin_amdgcn_perm(yb.z, ya.z, 0x05040100);
        yt[5][tid] = __builtin_amdgcn_perm(yb.z, ya.z, 0x07060302);
        yt[6][tid] = __builtin_amdgcn_perm(yb.w, ya.w, 0x05040100);
        yt[7][tid] = __builtin_amdgcn_perm(yb.w, ya.w, 0x07060302);
    }
    u32 l1p[5];
#pragma unroll
    for (int i = 0; i < 10; i += 2) {
        u32 dd = pk2(x[i] - m1, x[i + 1] - m1);
        l1p[i >> 1] = pk2(lo2f(dd) * r1, hi2f(dd) * r1);
    }
    __syncthreads();
    STAMP(1, 2);
    const u32x4 y4 = *(const u32x4*)(&yt[w][4 * lane]);
    const u32 y1 = yt[w][256 + lane];
    float y[10];
    unpack10(y4, y1, y);
    float h[10];
    u32 hp[5], op[5];
#pragma unroll
    for (int i = 0; i < 10; i += 2) {
        u32 l1 = l1p[i >> 1];
        u32 a = pk2(lo2f(l1) * y[i], hi2f(l1) * y[i + 1]);
        u32 hh = pk2(x[i] + lo2f(a), x[i + 1] + hi2f(a));
        hp[i >> 1] = hh;
        h[i] = lo2f(hh);
        h[i + 1] = hi2f(hh);
    }
    st16(hbuf + (size_t)t * D + 8 * lane, (u32x4){hp[0], hp[1], hp[2], hp[3]});
    {
        u32x4 tq = {(u32)__builtin_amdgcn_mov_dpp((int)hp[4], 0x00, 0xf, 0xf, true), (u32)__builtin_amdgcn_mov_dpp((int)hp[4], 0x55, 0xf, 0xf, true),
                    (u32)__builtin_amdgcn_mov_dpp((int)hp[4], 0xaa, 0xf, 0xf, true), (u32)__builtin_amdgcn_mov_dpp((int)hp[4], 0xff, 0xf, 0xf, true)};
        if ((lane & 3) == 0) st16(hbuf + (size_t)t * D + 512 + 2 * lane, tq);
    }
    float m2, s2;
    ln_stats(h, m2, s2);
    const float r2 = 1.0f / s2;
    float wr[10];
    unpack10(wa, wb, wr);
    float racc0 = 0.f, racc1 = 0.f;
#pragma unroll
    for (int i = 0; i < 10; i += 2) {
        u32 dd = pk2(h[i] - m2, h[i + 1] - m2);
        u32 l2 = pk2(lo2f(dd) * r2, hi2f(dd) * r2);
        racc0 += lo2f(l2) * wr[i];
        racc1 += hi2f(l2) * wr[i + 1];
        op[i >> 1] = pk2(h[i] + lo2f(l2), h[i + 1] + hi2f(l2));
    }
    st16o(out + (size_t)t * D + 8 * lane, (u32x4){op[0], op[1], op[2], op[3]});
    {
        u32x4 tq = {(u32)__builtin_amdgcn_mov_dpp((int)op[4], 0x00, 0xf, 0xf, true), (u32)__builtin_amdgcn_mov_dpp((int)op[4], 0x55, 0xf, 0xf, true),
                    (u32)__builtin_amdgcn_mov_dpp((int)op[4], 0xaa, 0xf, 0xf, true), (u32)__builtin_amdgcn_mov_dpp((int)op[4], 0xff, 0xf, 0xf, true)};
        if ((lane & 3) == 0) st16o(out + (size_t)t * D + 512 + 2 * lane, tq);
    }
    const float racc = wave_sum(racc0 + racc1);
    STAMP(1, 3);
    if (lane == 0) {
        u32 rb = pk2(racc, 0.f) & 0xffffu;
        u32 ord = (rb & 0x8000u) ? (rb ^ 0xffffu) : (rb ^ 0x8000u);
        keys[t] = (ord << 16) | (u32)(0xffff - t);
        *(f32x2*)(stats + 2 * t) = (f32x2){m2, r2};
    }
    STAMP(1, 4);
}

__device__ __forceinline__ float key_r(u32 key) {
    u32 ord = key >> 16;
    u32 rb = (ord & 0x8000u) ? (ord ^ 0x8000u) : (ord ^ 0xffffu);
    return __uint_as_float(rb << 16);
}

constexpr int XS = 648;
__global__ __launch_bounds__(512) void k3_gateup(const u32* __restrict__ keys, const u16* __restrict__ hbuf,
                                                 const float* __restrict__ stats, const u16* __restrict__ wgF,
                                                 const u16* __restrict__ wuF, u16* __restrict__ gu,
                                                 u32* __restrict__ selkeys, u16* __restrict__ selh,
                                                 u16* __restrict__ sell2) {
    __shared__ __attribute__((aligned(16))) u16 xs[8 * XS];
    __shared__ __attribute__((aligned(16))) float red[8][2][8][16];
    __shared__ u32 cand[64];
    const int b = blockIdx.x;
    const int slice = b;
    const int f0 = slice * 16;
    const int tid = threadIdx.x, lane = tid & 63;
    const int w = __builtin_amdgcn_readfirstlane(tid >> 6);
    STAMP(2, 0);
    u32 a[4];
    {
        u32x4 k0 = *(const u32x4*)(keys + tid * 4);
        a[0] = k0.x; a[1] = k0.y; a[2] = k0.z; a[3] = k0.w;
    }
    bf16x8 bg[3], bu[3];
#pragma unroll
    for (int i = 0; i < 3; ++i) {
        const int ks = (w + 8 * i < 20) ? (w + 8 * i) : w;
        bg[i] = *(const bf16x8*)(wgF + ((size_t)(slice * 20 + ks) * 64 + lane) * 8);
        bu[i] = *(const bf16x8*)(wuF + ((size_t)(slice * 20 + ks) * 64 + lane) * 8);
    }
#define CE(i, j) { u32 hi_ = a[i] > a[j] ? a[i] : a[j]; u32 lo_ = a[i] > a[j] ? a[j] : a[i]; a[i] = hi_; a[j] = lo_; }
    CE(0, 1) CE(2, 3) CE(0, 2) CE(1, 3) CE(1, 2)
#undef CE
    u32 mine = 0;
#pragma unroll
    for (int rd = 0; rd < 8; ++rd) {
        u32 m = wave_max_u32(a[0]);
        bool win = (a[0] == m);
        a[0] = win ? a[1] : a[0];
        a[1] = win ? a[2] : a[1];
        a[2] = win ? a[3] : a[2];
        a[3] = win ? 0u : a[3];
        if (lane == rd) mine = m;
    }
    if (lane < 8) cand[w * 8 + lane] = mine;
    STAMPN(2, 1);
    __syncthreads();
    STAMPN(2, 2);
    {
        u32 c = cand[lane];
        u32 key = 0;
        for (int rd = 0; rd <= w; ++rd) {
            key = wave_max_u32(c);
            c = (c == key) ? 0u : c;
        }
        const int ta = 0xffff - (int)(key & 0xffffu);
        const u32x4 ha = *(const u32x4*)(hbuf + (size_t)ta * D + 8 * lane);
        const u32 hb = *(const u32*)(hbuf + (size_t)ta * D + 512 + 2 * lane);
        const f32x2 st0 = *(const f32x2*)(stats + 2 * ta);
        STAMP(2, 3);
        const float m2 = st0.x, r2 = st0.y;
        float h[10];
        unpack10(ha, hb, h);
        u32 l2[5];
#pragma unroll
        for (int i = 0; i < 10; i += 2) {
            u32 dd = pk2(h[i] - m2, h[i + 1] - m2);
            l2[i >> 1] = pk2(lo2f(dd) * r2, hi2f(dd) * r2);
        }
        *(u32x4*)(xs + w * XS + 8 * lane) = (u32x4){l2[0], l2[1], l2[2], l2[3]};
        *(u32*)(xs + w * XS + 512 + 2 * lane) = l2[4];
        if (b == 0) {
            *(u32x4*)(selh + w * D + 8 * lane) = ha;
            *(u32*)(selh + w * D + 512 + 2 * lane) = hb;
            *(u32x4*)(sell2 + w * D + 8 * lane) = (u32x4){l2[0], l2[1], l2[2], l2[3]};
            *(u32*)(sell2 + w * D + 512 + 2 * lane) = l2[4];
            if (lane == 0) selkeys[w] = key;
        }
    }
    STAMPN(2, 4);
    __syncthreads();
    STAMPN(2, 5);
    {
        const int li = lane & 15, g = lane >> 4;
        f32x4 ag = {0.f, 0.f, 0.f, 0.f}, au = {0.f, 0.f, 0.f, 0.f};
#pragma unroll
        for (int s = 0; s < 2; ++s) {
            const int k0 = (w + 8 * s) * 32;
            bf16x8 af = *(const bf16x8*)(xs + (li & 7) * XS + k0 + 8 * g);
            ag = __builtin_amdgcn_mfma_f32_16x16x32_bf16(af, bg[s], ag, 0, 0, 0);
            au = __builtin_amdgcn_mfma_f32_16x16x32_bf16(af, bu[s], au, 0, 0, 0);
        }
        {
            const int k0 = ((w < 4) ? (w + 16) : w) * 32;
            bf16x8 af = *(const bf16x8*)(xs + (li & 7) * XS + k0 + 8 * g);
            f32x4 ag2 = __builtin_amdgcn_mfma_f32_16x16x32_bf16(af, bg[2], ag, 0, 0, 0);
            f32x4 au2 = __builtin_amdgcn_mfma_f32_16x16x32_bf16(af, bu[2], au, 0, 0, 0);
            if (w < 4) { ag = ag2; au = au2; }
        }
        if (g < 2) {
#pragma unroll
            for (int rr = 0; rr < 4; ++rr) {
                red[w][0][4 * g + rr][li] = ag[rr];
                red[w][1][4 * g + rr][li] = au[rr];
            }
        }
    }
    STAMPN(2, 6);
    __syncthreads();
    if (tid < 128) {
        const int tok = tid >> 4, col = tid & 15;
        float gs = ((red[0][0][tok][col] + red[1][0][tok][col]) + (red[2][0][tok][col] + red[3][0][tok][col])) +
                   ((red[4][0][tok][col] + red[5][0][tok][col]) + (red[6][0][tok][col] + red[7][0][tok][col]));
        float us = ((red[0][1][tok][col] + red[1][1][tok][col]) + (red[2][1][tok][col] + red[3][1][tok][col])) +
                   ((red[4][1][tok][col] + red[5][1][tok][col]) + (red[6][1][tok][col] + red[7][1][tok][col]));
        float gb = rbf(gs), ub = rbf(us);
        float sg = rbf(gb / (1.0f + __expf(-gb)));
        u32 o = pk2(sg * ub, 0.f);
        const int f = f0 + col;
        gu[((f >> 5) * 4 + ((f >> 3) & 3)) * 64 + tok * 8 + (f & 7)] = (u16)(o & 0xffffu);
    }
    STAMP(2, 7);
}

__global__ __launch_bounds__(512) void k4_down(const u16* __restrict__ gu, const u16* __restrict__ wdF,
                                               const u32* __restrict__ selkeys, const u16* __restrict__ selh,
                                               const u16* __restrict__ sell2, u16* __restrict__ out) {
    __shared__ __attribute__((aligned(16))) float red[8][8][16];
    const int slice = blockIdx.x;
    const int d0 = slice * 8;
    const int tid = threadIdx.x, lane = tid & 63;
    const int w = __builtin_amdgcn_readfirstlane(tid >> 6);
    const int li = lane & 15, g = lane >> 4;
    STAMP(3, 0);
    bf16x8 af[7], bf[7];
    {
        const u16* pa = gu + (w * 4 + g) * 64 + (li & 7) * 8;
        const u16* pb = wdF + ((size_t)(slice * 54 + w) * 32 + g * 8 + (li & 7)) * 8;
#pragma unroll
        for (int j = 0; j < 7; ++j) {
            const int jj = (j == 6 && w >= 6) ? 5 : j;
            af[j] = *(const bf16x8*)(pa + jj * 8 * 256);
            bf[j] = *(const bf16x8*)(pb + jj * 8 * 256);
        }
    }
    const int tok_ = (tid >> 3) & 7, col_ = tid & 7;
    const u32 key = selkeys[tok_];
    const float hv = __uint_as_float(((u32)selh[tok_ * D + d0 + col_]) << 16);
    const float l2v = __uint_as_float(((u32)sell2[tok_ * D + d0 + col_]) << 16);
    __builtin_amdgcn_sched_barrier(0);
    STAMP(3, 1);
    {
        f32x4 acc = {0.f, 0.f, 0.f, 0.f};
#pragma unroll
        for (int j = 0; j < 6; ++j) acc = __builtin_amdgcn_mfma_f32_16x16x32_bf16(af[j], bf[j], acc, 0, 0, 0);
        f32x4 acc2 = __builtin_amdgcn_mfma_f32_16x16x32_bf16(af[6], bf[6], acc, 0, 0, 0);
        if (w < 6) acc = acc2;
        if (g < 2) {
#pragma unroll
            for (int rr = 0; rr < 4; ++rr) red[w][4 * g + rr][li] = acc[rr];
        }
    }
    STAMP(3, 2);
    __syncthreads();
    if (tid < 64) {
        const int tok = tid >> 3, col = tid & 7;
        const int trow = 0xffff - (int)(key & 0xffffu);
        const float rv = key_r(key);
        const float rs = rbf(1.0f / (1.0f + __expf(-rv)));
        float s = ((red[0][tok][col] + red[1][tok][col]) + (red[2][tok][col] + red[3][tok][col])) +
                  ((red[4][tok][col] + red[5][tok][col]) + (red[6][tok][col] + red[7][tok][col]));
        float y = rbf(s);
        y = rbf(y * rs);
        float o1 = rbf(l2v + y);
        u32 o = pk2(hv + o1, 0.f);
        out[(size_t)trow * D + d0 + col] = (u16)(o & 0xffffu);
    }
    STAMP(3, 3);
}

extern "C" void kernel_launch(void* const* d_in, const int* in_sizes, int n_in, void* d_out, int out_size,
                              void* d_ws, size_t ws_size, hipStream_t stream) {
    const u16* q = (const u16*)d_in[0];
    const u16* k = (const u16*)d_in[1];
    const u16* v = (const u16*)d_in[2];
    const u16* shift_k = (const u16*)d_in[3];
    const u16* ssm_k = (const u16*)d_in[4];
    const u16* ssm_D = (const u16*)d_in[5];
    const u16* w_r = (const u16*)d_in[7];
    const u16* w_gate = (const u16*)d_in[8];
    const u16* w_up = (const u16*)d_in[9];
    const u16* w_down = (const u16*)d_in[10];
    u16* out = (u16*)d_out;
    char* ws = (char*)d_ws;
    const size_t TS = (size_t)S * D * 2;
    u16* uT = (u16*)(ws);
    u16* y2T = (u16*)(ws + TS);
    u16* hbuf = (u16*)(ws + 2 * TS);
    char* sm = ws + 3 * TS;
    float* stats = (float*)(sm);
    u32* keys = (u32*)(sm + 16384);
    u32* selkeys = (u32*)(sm + 24576);
    u16* selh = (u16*)(sm + 24576 + 256);
    u16* sell2 = (u16*)(sm + 24576 + 256 + 10240);
    u16* gu = (u16*)(sm + 24576 + 256 + 20480);
    const size_t WS_ = (size_t)D * F * 2;
    u16* wgF = (u16*)(sm + 131072);
    u16* wuF = (u16*)(sm + 131072 + WS_);
    u16* wdF = (u16*)(sm + 131072 + 2 * WS_);
#define PROF_MASK 0xff
    if (PROF_MASK & 1) k01_conv<<<CONV_WGS + SIDE_WGS, 512, 0, stream>>>(k, v, shift_k, ssm_k, ssm_D, y2T, w_gate, w_up, w_down, wgF, wuF, wdF, q);
    if (PROF_MASK & 4) k2_row<<<256, 512, 0, stream>>>(q, y2T, w_r, out, hbuf, stats, keys);
    if (PROF_MASK & 8) k3_gateup<<<108, 512, 0, stream>>>(keys, hbuf, stats, wgF, wuF, gu, selkeys, selh, sell2);
    if (PROF_MASK & 16) k4_down<<<80, 512, 0, stream>>>(gu, wdF, selkeys, selh, sell2, out);
}
```

```cpp
#include <hip/hip_runtime.h>
#include <hip/hip_bf16.h>

typedef unsigned short u16;
typedef unsigned int u32;
typedef __attribute__((ext_vector_type(8))) short bf16x8;
typedef __attribute__((ext_vector_type(4))) short s16x4;
typedef __attribute__((ext_vector_type(4))) float f32x4;
typedef __attribute__((ext_vector_type(2))) float f32x2;
typedef __attribute__((ext_vector_type(2))) __bf16 bfx2;
typedef __attribute__((ext_vector_type(4))) unsigned u32x4;
typedef __attribute__((ext_vector_type(2))) unsigned u32x2;

constexpr int S = 2048, D = 640, F = 1728, NSEL = 8;


__device__ __forceinline__ u32 pk2(float a, float b) {
    f32x2 v = {a, b};
    bfx2 r = __builtin_convertvector(v, bfx2);
    return __builtin_bit_cast(u32, r);
}
__device__ __forceinline__ float lo2f(u32 p) { return __uint_as_float(p << 16); }
__device__ __forceinline__ float hi2f(u32 p) { return __uint_as_float(p & 0xffff0000u); }
__device__ __forceinline__ float rbf(float a) { return lo2f(pk2(a, 0.f)); }

template <int CTRL, int RM> __device__ __forceinline__ int dpp_i(int v) {
    return __builtin_amdgcn_update_dpp(0, v, CTRL, RM, 0xf, true);
}
__device__ __forceinline__ float wave_sum(float v) {
    v += __int_as_float(dpp_i<0x111, 0xf>(__float_as_int(v)));
    v += __int_as_float(dpp_i<0x112, 0xf>(__float_as_int(v)));
    v += __int_as_float(dpp_i<0x114, 0xf>(__float_as_int(v)));
    v += __int_as_float(dpp_i<0x118, 0xf>(__float_as_int(v)));
    v += __int_as_float(dpp_i<0x142, 0xa>(__float_as_int(v)));
    v += __int_as_float(dpp_i<0x143, 0xc>(__float_as_int(v)));
    return __int_as_float(__builtin_amdgcn_readlane(__float_as_int(v), 63));
}
__device__ __forceinline__ u32 umax_(u32 a, u32 b) { return a > b ? a : b; }
__device__ __forceinline__ u32 wave_max_u32(u32 v) {
    v = umax_(v, (u32)dpp_i<0x111, 0xf>((int)v));
    v = umax_(v, (u32)dpp_i<0x112, 0xf>((int)v));
    v = umax_(v, (u32)dpp_i<0x114, 0xf>((int)v));
    v = umax_(v, (u32)dpp_i<0x118, 0xf>((int)v));
    v = umax_(v, (u32)dpp_i<0x142, 0xa>((int)v));
    v = umax_(v, (u32)dpp_i<0x143, 0xc>((int)v));
    return (u32)__builtin_amdgcn_readlane((int)v, 63);
}
#define KEEP4(val_) asm volatile("" ::"v"((val_).x), "v"((val_).y), "v"((val_).z), "v"((val_).w))

typedef __attribute__((address_space(3))) s16x4* lds_s16x4_ptr;
__device__ __forceinline__ s16x4 tr_read(const u16* p) {
    return __builtin_amdgcn_ds_read_tr16_b64_v4i16((lds_s16x4_ptr)p);
}
__device__ __forceinline__ u32 lds_addr(const u16* p) {
    return (u32)(size_t)(const __attribute__((address_space(3))) u16*)p;
}
__device__ __forceinline__ void st16(void* p, u32x4 v) { *(u32x4*)p = v; }
__device__ __forceinline__ void wave_lds_fence() {
    __builtin_amdgcn_fence(__ATOMIC_RELEASE, "wavefront");
    __builtin_amdgcn_wave_barrier();
    __builtin_amdgcn_fence(__ATOMIC_ACQUIRE, "wavefront");
}

__global__ __launch_bounds__(640) void k0_kv(const u16* __restrict__ k, const u16* __restrict__ v,
                                             const u16* __restrict__ shift_k, u16* __restrict__ uT,
                                             const u16* __restrict__ pf0, const u16* __restrict__ pf1) {
    __shared__ __attribute__((aligned(16))) u16 tile[80 * 72];
    const int b = blockIdx.x;
    const int ct = b & 7, rt = b >> 3;
    const int tid = threadIdx.x;
    const int rg = tid / 40, cp = tid - rg * 40;
    const int t0 = rt * 64 + rg * 4;
    const int c = ct * 40 + cp;
    u32 kk[7], vv[4];
#pragma unroll
    for (int i = 0; i < 7; ++i) {
        int t = t0 - 3 + i;
        kk[i] = *(const u32*)(k + (size_t)(t < 0 ? 0 : t) * D + 2 * c);
    }
#pragma unroll
    for (int i = 0; i < 4; ++i) vv[i] = *(const u32*)(v + (size_t)(t0 + i) * D + 2 * c);
    const u32x4 sw = *(const u32x4*)(shift_k + 8 * c);
    __builtin_amdgcn_sched_barrier(0);
    const u32x4 pa = *(const u32x4*)(pf0 + (size_t)(b * 640 + tid) * 8);
    const u32x4 pb = *(const u32x4*)(pf1 + (size_t)(b * 640 + tid) * 8);
    u32x4 pc0 = {0u, 0u, 0u, 0u}, pc1 = pc0;
    if (b < 8) {
        u32 alo, ahi, elo, ehi;
        asm volatile(
            "s_getpc_b64 s[20:21]\n\t"
            "s_add_u32 s20, s20, k1_conv@rel32@lo+4\n\t"
            "s_addc_u32 s21, s21, k1_conv@rel32@hi+12\n\t"
            "s_getpc_b64 s[22:23]\n\t"
            "s_add_u32 s22, s22, k9_end_marker@rel32@lo+4\n\t"
            "s_addc_u32 s23, s23, k9_end_marker@rel32@hi+12\n\t"
            "s_mov_b32 %0, s20\n\ts_mov_b32 %1, s21\n\ts_mov_b32 %2, s22\n\ts_mov_b32 %3, s23"
            : "=s"(alo), "=s"(ahi), "=s"(elo), "=s"(ehi)::"s20", "s21", "s22", "s23", "scc");
        const char* cb = (const char*)(((unsigned long long)ahi << 32) | alo);
        const char* ce = (const char*)(((unsigned long long)ehi << 32) | elo);
        const long long len = ce - cb;
        const long long o0 = (long long)tid * 16, o1 = (long long)(tid + 640) * 16;
        if (len > 0 && o0 + 16 <= len) pc0 = *(const u32x4*)(cb + o0);
        if (len > 0 && o1 + 16 <= len) pc1 = *(const u32x4*)(cb + o1);
    }
    __builtin_amdgcn_sched_barrier(0);
    if (t0 == 0) { kk[0] = 0u; kk[1] = 0u; kk[2] = 0u; }
    const float a0 = lo2f(sw.x), a1 = hi2f(sw.x), a2 = lo2f(sw.y), a3 = hi2f(sw.y);
    const float b0 = lo2f(sw.z), b1 = hi2f(sw.z), b2 = lo2f(sw.w), b3 = hi2f(sw.w);
    float ua[4], ub[4];
#pragma unroll
    for (int i = 0; i < 4; ++i) {
        u32 p0 = pk2(lo2f(kk[i]) * a3, hi2f(kk[i]) * b3);
        u32 p1 = pk2(lo2f(kk[i + 1]) * a2, hi2f(kk[i + 1]) * b2);
        u32 p2 = pk2(lo2f(kk[i + 2]) * a1, hi2f(kk[i + 2]) * b1);
        u32 p3 = pk2(lo2f(kk[i + 3]) * a0, hi2f(kk[i + 3]) * b0);
        u32 s = pk2(lo2f(p0) + lo2f(p1), hi2f(p0) + hi2f(p1));
        s = pk2(lo2f(s) + lo2f(p2), hi2f(s) + hi2f(p2));
        s = pk2(lo2f(s) + lo2f(p3), hi2f(s) + hi2f(p3));
        ua[i] = lo2f(s) * lo2f(vv[i]);
        ub[i] = hi2f(s) * hi2f(vv[i]);
    }
    u32x2 oa, ob;
    oa.x = pk2(ua[3], ua[2]); oa.y = pk2(ua[1], ua[0]);
    ob.x = pk2(ub[3], ub[2]); ob.y = pk2(ub[1], ub[0]);
    *(u32x2*)(tile + (2 * cp) * 72 + ((rg * 4) ^ 4)) = oa;
    *(u32x2*)(tile + (2 * cp + 1) * 72 + ((rg * 4) ^ 4)) = ob;
    __syncthreads();
    {
        const int ch = tid >> 3, pc = tid & 7;
        const u32x4 o = *(const u32x4*)(tile + ch * 72 + pc * 8);
        st16(uT + (size_t)(ct * 80 + ch) * S + rt * 64 + pc * 8, o);
    }
    KEEP4(pa);
    KEEP4(pb);
    KEEP4(pc0);
    KEEP4(pc1);
}

constexpr int UPAD = 256;
constexpr int KCS = 2080;
extern "C" __global__ __launch_bounds__(256, 3) void k1_conv(const u16* __restrict__ uT, const u16* __restrict__ ssm_k,
                                               const u16* __restrict__ ssm_D, u16* __restrict__ y2T,
                                               const u16* __restrict__ w_gate, const u16* __restrict__ w_up,
                                               const u16* __restrict__ w_down, u16* __restrict__ wgF,
                                               u16* __restrict__ wuF, u16* __restrict__ wdF) {
    __shared__ __attribute__((aligned(16))) u16 lds[UPAD + S + 12288 + 3 * 2048];
    u16* uL = lds;
    u16* kL = lds + UPAD + S;
    f32x4* part = (f32x4*)(lds + UPAD + S);
    const int tid = threadIdx.x;
    const int w = __builtin_amdgcn_readfirstlane(tid >> 6), lane = tid & 63;
    const int d = blockIdx.x;
    const u16* urow = uT + (size_t)d * S;
    const u16* krow = ssm_k + (size_t)d * S;
    const u32x4 z4 = {0u, 0u, 0u, 0u};
    u32x4 ur, kd;
    u32x2 ke;
    const int T = w * D + d;
    const int tm = T / 540, trem = T - tm * 540;
    const int tks = (tm < 2) ? trem / 27 : trem / 10;
    const int tcg = (tm < 2) ? trem - tks * 27 : trem - tks * 10;
    u16* tl = lds + UPAD + S + 12288 + w * 2048;
    ur = *(const u32x4*)(urow + tid * 8);
    kd = *(const u32x4*)(krow + tid * 8);
    ke = *(const u32x2*)(krow + (tid < 255 ? tid * 8 + 8 : 0));
    const u32 dpair = ((const u32*)ssm_D)[d >> 1];
    const float Dv = (d & 1) ? hi2f(dpair) : lo2f(dpair);
    if (tid == 255) ke = (u32x2){0u, 0u};
    *(u32x4*)(uL + UPAD + tid * 8) = ur;
    if (tid < 32) *(u32x4*)(uL + tid * 8) = z4;
    {
        u32 d0 = kd.x, d1 = kd.y, d2 = kd.z, d3 = kd.w, d4 = ke.x, d5 = ke.y;
        u32 s0 = __builtin_amdgcn_alignbit(d1, d0, 16);
        u32 s1 = __builtin_amdgcn_alignbit(d2, d1, 16);
        u32 s2 = __builtin_amdgcn_alignbit(d3, d2, 16);
        u32 s3 = __builtin_amdgcn_alignbit(d4, d3, 16);
        u32 s4 = __builtin_amdgcn_alignbit(d5, d4, 16);
        u32x4 c0 = {d0, d1, d2, d3}, c1 = {s0, s1, s2, s3}, c2 = {d1, d2, d3, d4}, c3 = {s1, s2, s3, s4};
        const int Y = tid + 2;
        *(u32x4*)(kL + 0 * KCS + Y * 8) = c0;
        *(u32x4*)(kL + 1 * KCS + Y * 8) = c1;
        *(u32x4*)(kL + 2 * KCS + Y * 8) = c2;
        *(u32x4*)(kL + 3 * KCS + Y * 8) = c3;
        if (tid == 0) {
            u32x4 e1 = {0u, 0u, 0u, d0 << 16}, e2 = {0u, 0u, 0u, d0}, e3 = {0u, 0u, d0 << 16, s0};
            *(u32x4*)(kL + 0 * KCS + 0) = z4; *(u32x4*)(kL + 0 * KCS + 8) = z4;
            *(u32x4*)(kL + 1 * KCS + 0) = z4; *(u32x4*)(kL + 1 * KCS + 8) = e1;
            *(u32x4*)(kL + 2 * KCS + 0) = z4; *(u32x4*)(kL + 2 * KCS + 8) = e2;
            *(u32x4*)(kL + 3 * KCS + 0) = z4; *(u32x4*)(kL + 3 * KCS + 8) = e3;
        }
    }
    __syncthreads();
    if (T < 1620) {
        const u16* src = (tm < 2) ? ((tm ? w_up : w_gate) + (size_t)(tks * 32) * F + tcg * 64)
                                  : (w_down + (size_t)(tks * 32) * D + tcg * 64);
        const int ldm = (tm < 2) ? F : D;
        const u16* g0 = src + (size_t)(lane >> 3) * ldm + (lane & 7) * 8;
        const u16* g1 = g0 + (size_t)8 * ldm;
        const u16* g2 = g0 + (size_t)16 * ldm;
        const u16* g3 = g0 + (size_t)24 * ldm;
        const u32 l0 = __builtin_amdgcn_readfirstlane(lds_addr(tl));
        u32 keep;
        asm volatile(
            "s_mov_b32 %0, m0\n\t"
            "s_mov_b32 m0, %5\n\ts_nop 0\n\tglobal_load_lds_dwordx4 %1, off\n\t"
            "s_add_u32 m0, %5, 0x400\n\ts_nop 0\n\tglobal_load_lds_dwordx4 %2, off\n\t"
            "s_add_u32 m0, %5, 0x800\n\ts_nop 0\n\tglobal_load_lds_dwordx4 %3, off\n\t"
            "s_add_u32 m0, %5, 0xc00\n\ts_nop 0\n\tglobal_load_lds_dwordx4 %4, off\n\t"
            "s_mov_b32 m0, %0"
            : "=&s"(keep)
            : "v"(g0), "v"(g1), "v"(g2), "v"(g3), "s"(l0)
            : "memory", "scc");
    }
    const int li = lane & 15, g = lane >> 4;
    const u16* aBase = kL + ((li + 1) & 3) * KCS + ((li + 1) & ~3) + 8 * g + 64 * w;
    const u16* bBase = uL + UPAD + 16 * li + 8 - 8 * g - 64 * w;
    f32x4 acc[8];
#pragma unroll
    for (int c = 0; c < 8; ++c) acc[c] = (f32x4){0.f, 0.f, 0.f, 0.f};
    const u32 aAddr = lds_addr(aBase);
    const u32 bAddr = lds_addr(bBase - 32);
    s16x4 alo[2][8], ahi[2][8];
    bf16x8 bf[2][8];
#define LOAD_FRAGS(r)                                                                                   \
    _Pragma("unroll") for (int n = 0; n < 8; ++n) {                                                     \
        asm volatile("ds_read_b64 %0, %1 offset:%2" : "=v"(alo[r][n]) : "v"(aAddr), "i"(64 * (8 * n + r))); \
        asm volatile("ds_read_b64 %0, %1 offset:%2" : "=v"(ahi[r][n]) : "v"(aAddr), "i"(64 * (8 * n + r) + 8)); \
        asm volatile("ds_read_b128 %0, %1 offset:%2" : "=v"(bf[r][n]) : "v"(bAddr), "i"(64 * (8 * n - r) + 64)); \
    }
#define MFMA_ALL(r)                                                                                     \
    _Pragma("unroll") for (int sum = 0; sum < 8; ++sum) _Pragma("unroll") for (int n = 0; n <= sum; ++n) \
        acc[sum] = __builtin_amdgcn_mfma_f32_16x16x32_bf16(                                             \
            __builtin_shufflevector(alo[r][n], ahi[r][n], 0, 1, 2, 3, 4, 5, 6, 7), bf[r][sum - n], acc[sum], 0, 0, 0);
    LOAD_FRAGS(0)
    asm volatile("s_waitcnt lgkmcnt(0)" ::: "memory");
    __builtin_amdgcn_sched_barrier(0);
    LOAD_FRAGS(1)
    __builtin_amdgcn_sched_barrier(0);
    MFMA_ALL(0)
    __builtin_amdgcn_sched_barrier(0);
    asm volatile("s_waitcnt lgkmcnt(0)" ::: "memory");
    __builtin_amdgcn_sched_barrier(0);
    MFMA_ALL(1)
#undef LOAD_FRAGS
#undef MFMA_ALL
    __syncthreads();
#pragma unroll
    for (int c = 0; c < 8; ++c) {
        const int o = c >> 1;
        if (o != w) part[((o * 3 + ((w - o - 1) & 3)) * 2 + (c & 1)) * 64 + lane] = acc[c];
    }
    __syncthreads();
    if (T < 1620) {
        asm volatile("s_waitcnt vmcnt(0)" ::: "memory");
#pragma unroll
        for (int j = 0; j < 4; ++j) {
            const u16* rp = tl + (8 * g + (li >> 2)) * 64 + 16 * j + 4 * (li & 3);
            s16x4 b0 = tr_read(rp), b1 = tr_read(rp + 4 * 64);
            bf16x8 fr = __builtin_shufflevector(b0, b1, 0, 1, 2, 3, 4, 5, 6, 7);
            u16* dst;
            if (tm < 2) dst = (tm ? wuF : wgF) + ((size_t)((tcg * 4 + j) * 20 + tks) * 64 + lane) * 8;
            else dst = wdF + ((size_t)((tcg * 8 + 2 * j + (li >> 3)) * 54 + tks) * 32 + g * 8 + (li & 7)) * 8;
            asm volatile("global_store_dwordx4 %0, %1, off sc1\n\ts_nop 1" ::"v"(dst), "v"(fr) : "memory");
        }
    }
    u16* yrow = y2T + (size_t)d * S;
    u32x2 ov[2];
#pragma unroll
    for (int j = 0; j < 2; ++j) {
        const int c = 2 * w + j;
        f32x4 own = acc[j];
#pragma unroll
        for (int q = 1; q < 4; ++q) own = (w == q) ? acc[2 * q + j] : own;
        f32x4 a0 = part[((w * 3 + 0) * 2 + j) * 64 + lane], a1 = part[((w * 3 + 1) * 2 + j) * 64 + lane];
        f32x4 a2 = part[((w * 3 + 2) * 2 + j) * 64 + lane];
        f32x4 a = (a0 + a1) + (a2 + own);
        const int tb = 256 * c + 16 * li + 8 * (g >> 1);
        u32x2 uu = *(const u32x2*)(uL + UPAD + tb + 4 * (1 - (g & 1)));
        float u3 = lo2f(uu.x), u2 = hi2f(uu.x), u1 = lo2f(uu.y), u0 = hi2f(uu.y);
        u32 y01 = pk2(a[0], a[1]);
        u32 y23 = pk2(a[2], a[3]);
        u32 w01 = pk2(u0 * Dv, u1 * Dv);
        u32 w23 = pk2(u2 * Dv, u3 * Dv);
        ov[j].x = pk2(lo2f(y01) + lo2f(w01), hi2f(y01) + hi2f(w01));
        ov[j].y = pk2(lo2f(y23) + lo2f(w23), hi2f(y23) + hi2f(w23));
    }
    {
        auto rx = __builtin_amdgcn_permlane16_swap(ov[0].x, ov[1].x, false, false);
        auto ry = __builtin_amdgcn_permlane16_swap(ov[0].y, ov[1].y, false, false);
        u32x4 o = {rx[0], ry[0], rx[1], ry[1]};
        st16(yrow + 256 * (2 * w + (g & 1)) + 16 * li + 8 * (g >> 1), o);
    }
    asm volatile("s_waitcnt vmcnt(0)" ::: "memory");
}

__device__ __forceinline__ void unpack10(const u32x4& a, u32 b, float* f) {
    f[0] = lo2f(a.x); f[1] = hi2f(a.x); f[2] = lo2f(a.y); f[3] = hi2f(a.y);
    f[4] = lo2f(a.z); f[5] = hi2f(a.z); f[6] = lo2f(a.w); f[7] = hi2f(a.w);
    f[8] = lo2f(b); f[9] = hi2f(b);
}
__device__ __forceinline__ void ln_stats(const float* x, float& m_bf, float& s_bf) {
    float s = ((x[0] + x[1]) + (x[2] + x[3])) + ((x[4] + x[5]) + (x[6] + x[7])) + (x[8] + x[9]);
    s = wave_sum(s);
    const float mean = s * (1.0f / D);
    float q0 = 0.f, q1 = 0.f;
#pragma unroll
    for (int i = 0; i < 10; i += 2) {
        float d0 = x[i] - mean, d1 = x[i + 1] - mean;
        q0 += d0 * d0;
        q1 += d1 * d1;
    }
    float q = wave_sum(q0 + q1);
    const float var = q * (1.0f / D);
    m_bf = rbf(mean);
    float vb = rbf(var);
    float ve = rbf(vb + 1e-5f);
    s_bf = rbf(sqrtf(ve));
}

__global__ __launch_bounds__(512) void k2_row(const u16* __restrict__ q, const u16* __restrict__ y2T,
                                              const u16* __restrict__ w_r, u16* __restrict__ out,
                                              u16* __restrict__ hbuf, float* __restrict__ stats,
                                              u32* __restrict__ keys) {
    __shared__ __attribute__((aligned(16))) u32 yt[8][320];
    const int b = blockIdx.x;
    const int tb = (b & 7) * 32 + (b >> 3);
    const int t0 = tb * 8;
    const int tid = threadIdx.x;
    const int w = tid >> 6, lane = tid & 63;
    const int t = t0 + w;
    const u32x4 xa = *(const u32x4*)(q + (size_t)t * D + 8 * lane);
    const u32 xb = *(const u32*)(q + (size_t)t * D + 512 + 2 * lane);
    u32x4 ya = {0u, 0u, 0u, 0u}, yb = {0u, 0u, 0u, 0u};
    if (tid < 320) {
        ya = *(const u32x4*)(y2T + (size_t)(2 * tid) * S + t0);
        yb = *(const u32x4*)(y2T + (size_t)(2 * tid + 1) * S + t0);
    }
    const u32x4 wa = *(const u32x4*)(w_r + 8 * lane);
    const u32 wb = *(const u32*)(w_r + 512 + 2 * lane);
    float x[10];
    unpack10(xa, xb, x);
    float m1, s1;
    ln_stats(x, m1, s1);
    const float r1 = 1.0f / s1;
    if (tid < 320) {
        yt[0][tid] = __builtin_amdgcn_perm(yb.x, ya.x, 0x05040100);
        yt[1][tid] = __builtin_amdgcn_perm(yb.x, ya.x, 0x07060302);
        yt[2][tid] = __builtin_amdgcn_perm(yb.y, ya.y, 0x05040100);
        yt[3][tid] = __builtin_amdgcn_perm(yb.y, ya.y, 0x07060302);
        yt[4][tid] = __builtin_amdgcn_perm(yb.z, ya.z, 0x05040100);
        yt[5][tid] = __builtin_amdgcn_perm(yb.z, ya.z, 0x07060302);
        yt[6][tid] = __builtin_amdgcn_perm(yb.w, ya.w, 0x05040100);
        yt[7][tid] = __builtin_amdgcn_perm(yb.w, ya.w, 0x07060302);
    }
    u32 l1p[5];
#pragma unroll
    for (int i = 0; i < 10; i += 2) {
        u32 dd = pk2(x[i] - m1, x[i + 1] - m1);
        l1p[i >> 1] = pk2(lo2f(dd) * r1, hi2f(dd) * r1);
    }
    __syncthreads();
    const u32x4 y4 = *(const u32x4*)(&yt[w][4 * lane]);
    const u32 y1 = yt[w][256 + lane];
    float y[10];
    unpack10(y4, y1, y);
    float h[10];
    u32 hp[5], op[5];
#pragma unroll
    for (int i = 0; i < 10; i += 2) {
        u32 l1 = l1p[i >> 1];
        u32 a = pk2(lo2f(l1) * y[i], hi2f(l1) * y[i + 1]);
        u32 hh = pk2(x[i] + lo2f(a), x[i + 1] + hi2f(a));
        hp[i >> 1] = hh;
        h[i] = lo2f(hh);
        h[i + 1] = hi2f(hh);
    }
    st16(hbuf + (size_t)t * D + 8 * lane, (u32x4){hp[0], hp[1], hp[2], hp[3]});
    {
        u32x4 tq = {(u32)__builtin_amdgcn_mov_dpp((int)hp[4], 0x00, 0xf, 0xf, true), (u32)__builtin_amdgcn_mov_dpp((int)hp[4], 0x55, 0xf, 0xf, true),
                    (u32)__builtin_amdgcn_mov_dpp((int)hp[4], 0xaa, 0xf, 0xf, true), (u32)__builtin_amdgcn_mov_dpp((int)hp[4], 0xff, 0xf, 0xf, true)};
        if ((lane & 3) == 0) st16(hbuf + (size_t)t * D + 512 + 2 * lane, tq);
    }
    float m2, s2;
    ln_stats(h, m2, s2);
    const float r2 = 1.0f / s2;
    float wr[10];
    unpack10(wa, wb, wr);
    float racc0 = 0.f, racc1 = 0.f;
#pragma unroll
    for (int i = 0; i < 10; i += 2) {
        u32 dd = pk2(h[i] - m2, h[i + 1] - m2);
        u32 l2 = pk2(lo2f(dd) * r2, hi2f(dd) * r2);
        racc0 += lo2f(l2) * wr[i];
        racc1 += hi2f(l2) * wr[i + 1];
        op[i >> 1] = pk2(h[i] + lo2f(l2), h[i + 1] + hi2f(l2));
    }
    st16(out + (size_t)t * D + 8 * lane, (u32x4){op[0], op[1], op[2], op[3]});
    {
        u32x4 tq = {(u32)__builtin_amdgcn_mov_dpp((int)op[4], 0x00, 0xf, 0xf, true), (u32)__builtin_amdgcn_mov_dpp((int)op[4], 0x55, 0xf, 0xf, true),
                    (u32)__builtin_amdgcn_mov_dpp((int)op[4], 0xaa, 0xf, 0xf, true), (u32)__builtin_amdgcn_mov_dpp((int)op[4], 0xff, 0xf, 0xf, true)};
        if ((lane & 3) == 0) st16(out + (size_t)t * D + 512 + 2 * lane, tq);
    }
    const float racc = wave_sum(racc0 + racc1);
    if (lane == 0) {
        u32 rb = pk2(racc, 0.f) & 0xffffu;
        u32 ord = (rb & 0x8000u) ? (rb ^ 0xffffu) : (rb ^ 0x8000u);
        keys[t] = (ord << 16) | (u32)(0xffff - t);
        *(f32x2*)(stats + 2 * t) = (f32x2){m2, r2};
    }
}

__device__ __forceinline__ float key_r(u32 key) {
    u32 ord = key >> 16;
    u32 rb = (ord & 0x8000u) ? (ord ^ 0x8000u) : (ord ^ 0xffffu);
    return __uint_as_float(rb << 16);
}

constexpr int XS = 648;
__global__ __launch_bounds__(512) void k3_gateup(const u32* __restrict__ keys, const u16* __restrict__ hbuf,
                                                 const float* __restrict__ stats, const u16* __restrict__ wgF,
                                                 const u16* __restrict__ wuF, u16* __restrict__ gu,
                                                 u32* __restrict__ selkeys, u16* __restrict__ selh,
                                                 u16* __restrict__ sell2) {
    __shared__ __attribute__((aligned(16))) u16 xs[8 * XS];
    __shared__ __attribute__((aligned(16))) float red[8][2][8][16];
    __shared__ u32 cand[64];
    const int b = blockIdx.x;
    const int slice = b;
    const int f0 = slice * 16;
    const int tid = threadIdx.x, lane = tid & 63;
    const int w = __builtin_amdgcn_readfirstlane(tid >> 6);
    u32 a[4];
    {
        u32x4 k0 = *(const u32x4*)(keys + tid * 4);
        a[0] = k0.x; a[1] = k0.y; a[2] = k0.z; a[3] = k0.w;
    }
    bf16x8 bg[3], bu[3];
#pragma unroll
    for (int i = 0; i < 3; ++i) {
        const int ks = (w + 8 * i < 20) ? (w + 8 * i) : w;
        bg[i] = *(const bf16x8*)(wgF + ((size_t)(slice * 20 + ks) * 64 + lane) * 8);
        bu[i] = *(const bf16x8*)(wuF + ((size_t)(slice * 20 + ks) * 64 + lane) * 8);
    }
#define CE(i, j) { u32 hi_ = a[i] > a[j] ? a[i] : a[j]; u32 lo_ = a[i] > a[j] ? a[j] : a[i]; a[i] = hi_; a[j] = lo_; }
    CE(0, 1) CE(2, 3) CE(0, 2) CE(1, 3) CE(1, 2)
#undef CE
    u32 mine = 0;
#pragma unroll
    for (int rd = 0; rd < 8; ++rd) {
        u32 m = wave_max_u32(a[0]);
        bool win = (a[0] == m);
        a[0] = win ? a[1] : a[0];
        a[1] = win ? a[2] : a[1];
        a[2] = win ? a[3] : a[2];
        a[3] = win ? 0u : a[3];
        if (lane == rd) mine = m;
    }
    if (lane < 8) cand[w * 8 + lane] = mine;
    __syncthreads();
    {
        u32 c = cand[lane];
        u32 key = 0;
        for (int rd = 0; rd <= w; ++rd) {
            key = wave_max_u32(c);
            c = (c == key) ? 0u : c;
        }
        const int ta = 0xffff - (int)(key & 0xffffu);
        const u32x4 ha = *(const u32x4*)(hbuf + (size_t)ta * D + 8 * lane);
        const u32 hb = *(const u32*)(hbuf + (size_t)ta * D + 512 + 2 * lane);
        const f32x2 st0 = *(const f32x2*)(stats + 2 * ta);
        const float m2 = st0.x, r2 = st0.y;
        float h[10];
        unpack10(ha, hb, h);
        u32 l2[5];
#pragma unroll
        for (int i = 0; i < 10; i += 2) {
            u32 dd = pk2(h[i] - m2, h[i + 1] - m2);
            l2[i >> 1] = pk2(lo2f(dd) * r2, hi2f(dd) * r2);
        }
        *(u32x4*)(xs + w * XS + 8 * lane) = (u32x4){l2[0], l2[1], l2[2], l2[3]};
        *(u32*)(xs + w * XS + 512 + 2 * lane) = l2[4];
        if (b == 0) {
            *(u32x4*)(selh + w * D + 8 * lane) = ha;
            *(u32*)(selh + w * D + 512 + 2 * lane) = hb;
            *(u32x4*)(sell2 + w * D + 8 * lane) = (u32x4){l2[0], l2[1], l2[2], l2[3]};
            *(u32*)(sell2 + w * D + 512 + 2 * lane) = l2[4];
            if (lane == 0) selkeys[w] = key;
        }
    }
    __syncthreads();
    {
        const int li = lane & 15, g = lane >> 4;
        f32x4 ag = {0.f, 0.f, 0.f, 0.f}, au = {0.f, 0.f, 0.f, 0.f};
#pragma unroll
        for (int s = 0; s < 2; ++s) {
            const int k0 = (w + 8 * s) * 32;
            bf16x8 af = *(const bf16x8*)(xs + (li & 7) * XS + k0 + 8 * g);
            ag = __builtin_amdgcn_mfma_f32_16x16x32_bf16(af, bg[s], ag, 0, 0, 0);
            au = __builtin_amdgcn_mfma_f32_16x16x32_bf16(af, bu[s], au, 0, 0, 0);
        }
        {
            const int k0 = ((w < 4) ? (w + 16) : w) * 32;
            bf16x8 af = *(const bf16x8*)(xs + (li & 7) * XS + k0 + 8 * g);
            f32x4 ag2 = __builtin_amdgcn_mfma_f32_16x16x32_bf16(af, bg[2], ag, 0, 0, 0);
            f32x4 au2 = __builtin_amdgcn_mfma_f32_16x16x32_bf16(af, bu[2], au, 0, 0, 0);
            if (w < 4) { ag = ag2; au = au2; }
        }
        if (g < 2) {
#pragma unroll
            for (int rr = 0; rr < 4; ++rr) {
                red[w][0][4 * g + rr][li] = ag[rr];
                red[w][1][4 * g + rr][li] = au[rr];
            }
        }
    }
    __syncthreads();
    if (tid < 128) {
        const int tok = tid >> 4, col = tid & 15;
        float gs = ((red[0][0][tok][col] + red[1][0][tok][col]) + (red[2][0][tok][col] + red[3][0][tok][col])) +
                   ((red[4][0][tok][col] + red[5][0][tok][col]) + (red[6][0][tok][col] + red[7][0][tok][col]));
        float us = ((red[0][1][tok][col] + red[1][1][tok][col]) + (red[2][1][tok][col] + red[3][1][tok][col])) +
                   ((red[4][1][tok][col] + red[5][1][tok][col]) + (red[6][1][tok][col] + red[7][1][tok][col]));
        float gb = rbf(gs), ub = rbf(us);
        float sg = rbf(gb / (1.0f + __expf(-gb)));
        u32 o = pk2(sg * ub, 0.f);
        const int f = f0 + col;
        gu[((f >> 5) * 4 + ((f >> 3) & 3)) * 64 + tok * 8 + (f & 7)] = (u16)(o & 0xffffu);
    }
}

__global__ __launch_bounds__(512) void k4_down(const u16* __restrict__ gu, const u16* __restrict__ wdF,
                                               const u32* __restrict__ selkeys, const u16* __restrict__ selh,
                                               const u16* __restrict__ sell2, u16* __restrict__ out) {
    __shared__ __attribute__((aligned(16))) float red[8][8][16];
    const int slice = blockIdx.x;
    const int d0 = slice * 8;
    const int tid = threadIdx.x, lane = tid & 63;
    const int w = __builtin_amdgcn_readfirstlane(tid >> 6);
    const int li = lane & 15, g = lane >> 4;
    bf16x8 af[7], bf[7];
    {
        const u16* pa = gu + (w * 4 + g) * 64 + (li & 7) * 8;
        const u16* pb = wdF + ((size_t)(slice * 54 + w) * 32 + g * 8 + (li & 7)) * 8;
#pragma unroll
        for (int j = 0; j < 7; ++j) {
            const int jj = (j == 6 && w >= 6) ? 5 : j;
            af[j] = *(const bf16x8*)(pa + jj * 8 * 256);
            bf[j] = *(const bf16x8*)(pb + jj * 8 * 256);
        }
    }
    const int tok_ = (tid >> 3) & 7, col_ = tid & 7;
    const u32 key = selkeys[tok_];
    const float hv = __uint_as_float(((u32)selh[tok_ * D + d0 + col_]) << 16);
    const float l2v = __uint_as_float(((u32)sell2[tok_ * D + d0 + col_]) << 16);
    __builtin_amdgcn_sched_barrier(0);
    {
        f32x4 acc = {0.f, 0.f, 0.f, 0.f};
#pragma unroll
        for (int j = 0; j < 6; ++j) acc = __builtin_amdgcn_mfma_f32_16x16x32_bf16(af[j], bf[j], acc, 0, 0, 0);
        f32x4 acc2 = __builtin_amdgcn_mfma_f32_16x16x32_bf16(af[6], bf[6], acc, 0, 0, 0);
        if (w < 6) acc = acc2;
        if (g < 2) {
#pragma unroll
            for (int rr = 0; rr < 4; ++rr) red[w][4 * g + rr][li] = acc[rr];
        }
    }
    __syncthreads();
    if (tid < 64) {
        const int tok = tid >> 3, col = tid & 7;
        const int trow = 0xffff - (int)(key & 0xffffu);
        const float rv = key_r(key);
        const float rs = rbf(1.0f / (1.0f + __expf(-rv)));
        float s = ((red[0][tok][col] + red[1][tok][col]) + (red[2][tok][col] + red[3][tok][col])) +
                  ((red[4][tok][col] + red[5][tok][col]) + (red[6][tok][col] + red[7][tok][col]));
        float y = rbf(s);
        y = rbf(y * rs);
        float o1 = rbf(l2v + y);
        u32 o = pk2(hv + o1, 0.f);
        out[(size_t)trow * D + d0 + col] = (u16)(o & 0xffffu);
    }
}

extern "C" __global__ void k9_end_marker(int* p) { if (p == nullptr) *p = 0; }

extern "C" void kernel_launch(void* const* d_in, const int* in_sizes, int n_in, void* d_out, int out_size,
                              void* d_ws, size_t ws_size, hipStream_t stream) {
    const u16* q = (const u16*)d_in[0];
    const u16* k = (const u16*)d_in[1];
    const u16* v = (const u16*)d_in[2];
    const u16* shift_k = (const u16*)d_in[3];
    const u16* ssm_k = (const u16*)d_in[4];
    const u16* ssm_D = (const u16*)d_in[5];
    const u16* w_r = (const u16*)d_in[7];
    const u16* w_gate = (const u16*)d_in[8];
    const u16* w_up = (const u16*)d_in[9];
    const u16* w_down = (const u16*)d_in[10];
    u16* out = (u16*)d_out;
    char* ws = (char*)d_ws;
    const size_t TS = (size_t)S * D * 2;
    u16* uT = (u16*)(ws);
    u16* y2T = (u16*)(ws + TS);
    u16* hbuf = (u16*)(ws);
    char* sm = ws + 2 * TS;
    float* stats = (float*)(sm);
    u32* keys = (u32*)(sm + 16384);
    u32* selkeys = (u32*)(sm + 24576);
    u16* selh = (u16*)(sm + 24576 + 256);
    u16* sell2 = (u16*)(sm + 24576 + 256 + 10240);
    u16* gu = (u16*)(sm + 24576 + 256 + 20480);
    const size_t WS_ = (size_t)D * F * 2;
    u16* wgF = (u16*)(sm + 131072);
    u16* wuF = (u16*)(sm + 131072 + WS_);
    u16* wdF = (u16*)(sm + 131072 + 2 * WS_);
    k0_kv<<<256, 640, 0, stream>>>(k, v, shift_k, uT, ssm_k, q);
    k1_conv<<<D, 256, 0, stream>>>(uT, ssm_k, ssm_D, y2T, w_gate, w_up, w_down, wgF, wuF, wdF);
    k2_row<<<256, 512, 0, stream>>>(q, y2T, w_r, out, hbuf, stats, keys);
    k3_gateup<<<108, 512, 0, stream>>>(keys, hbuf, stats, wgF, wuF, gu, selkeys, selh, sell2);
    k4_down<<<80, 512, 0, stream>>>(gu, wdF, selkeys, selh, sell2, out);
}
```

```cpp
#include <hip/hip_runtime.h>
#include <hip/hip_bf16.h>

typedef unsigned short u16;
typedef unsigned int u32;
typedef __attribute__((ext_vector_type(8))) short bf16x8;
typedef __attribute__((ext_vector_type(4))) short s16x4;
typedef __attribute__((ext_vector_type(4))) float f32x4;
typedef __attribute__((ext_vector_type(2))) float f32x2;
typedef __attribute__((ext_vector_type(2))) __bf16 bfx2;
typedef __attribute__((ext_vector_type(4))) unsigned u32x4;
typedef __attribute__((ext_vector_type(2))) unsigned u32x2;

constexpr int S = 2048, D = 640, F = 1728, NSEL = 8;


__device__ __forceinline__ u32 pk2(float a, float b) {
    f32x2 v = {a, b};
    bfx2 r = __builtin_convertvector(v, bfx2);
    return __builtin_bit_cast(u32, r);
}
__device__ __forceinline__ float lo2f(u32 p) { return __uint_as_float(p << 16); }
__device__ __forceinline__ float hi2f(u32 p) { return __uint_as_float(p & 0xffff0000u); }
__device__ __forceinline__ float rbf(float a) { return lo2f(pk2(a, 0.f)); }

template <int CTRL, int RM> __device__ __forceinline__ int dpp_i(int v) {
    return __builtin_amdgcn_update_dpp(0, v, CTRL, RM, 0xf, true);
}
__device__ __forceinline__ float wave_sum(float v) {
    v += __int_as_float(dpp_i<0x111, 0xf>(__float_as_int(v)));
    v += __int_as_float(dpp_i<0x112, 0xf>(__float_as_int(v)));
    v += __int_as_float(dpp_i<0x114, 0xf>(__float_as_int(v)));
    v += __int_as_float(dpp_i<0x118, 0xf>(__float_as_int(v)));
    v += __int_as_float(dpp_i<0x142, 0xa>(__float_as_int(v)));
    v += __int_as_float(dpp_i<0x143, 0xc>(__float_as_int(v)));
    return __int_as_float(__builtin_amdgcn_readlane(__float_as_int(v), 63));
}
__device__ __forceinline__ u32 umax_(u32 a, u32 b) { return a > b ? a : b; }
__device__ __forceinline__ u32 wave_max_u32(u32 v) {
    v = umax_(v, (u32)dpp_i<0x111, 0xf>((int)v));
    v = umax_(v, (u32)dpp_i<0x112, 0xf>((int)v));
    v = umax_(v, (u32)dpp_i<0x114, 0xf>((int)v));
    v = umax_(v, (u32)dpp_i<0x118, 0xf>((int)v));
    v = umax_(v, (u32)dpp_i<0x142, 0xa>((int)v));
    v = umax_(v, (u32)dpp_i<0x143, 0xc>((int)v));
    return (u32)__builtin_amdgcn_readlane((int)v, 63);
}
#define KEEP4(val_) asm volatile("" ::"v"((val_).x), "v"((val_).y), "v"((val_).z), "v"((val_).w))

typedef __attribute__((address_space(3))) s16x4* lds_s16x4_ptr;
__device__ __forceinline__ s16x4 tr_read(const u16* p) {
    return __builtin_amdgcn_ds_read_tr16_b64_v4i16((lds_s16x4_ptr)p);
}
__device__ __forceinline__ u32 lds_addr(const u16* p) {
    return (u32)(size_t)(const __attribute__((address_space(3))) u16*)p;
}
__device__ __forceinline__ void st16(void* p, u32x4 v) { *(u32x4*)p = v; }
__device__ __forceinline__ void wave_lds_fence() {
    __builtin_amdgcn_fence(__ATOMIC_RELEASE, "wavefront");
    __builtin_amdgcn_wave_barrier();
    __builtin_amdgcn_fence(__ATOMIC_ACQUIRE, "wavefront");
}

__global__ __launch_bounds__(640) void k0_kv(const u16* __restrict__ k, const u16* __restrict__ v,
                                             const u16* __restrict__ shift_k, u16* __restrict__ uT,
                                             const u16* __restrict__ pf0, const u16* __restrict__ pf1) {
    __shared__ __attribute__((aligned(16))) u16 tile[80 * 72];
    const int b = blockIdx.x;
    const int ct = b & 7, rt = b >> 3;
    const int tid = threadIdx.x;
    const int rg = tid / 40, cp = tid - rg * 40;
    const int t0 = rt * 64 + rg * 4;
    const int c = ct * 40 + cp;
    u32 kk[7], vv[4];
#pragma unroll
    for (int i = 0; i < 7; ++i) {
        int t = t0 - 3 + i;
        kk[i] = *(const u32*)(k + (size_t)(t < 0 ? 0 : t) * D + 2 * c);
    }
#pragma unroll
    for (int i = 0; i < 4; ++i) vv[i] = *(const u32*)(v + (size_t)(t0 + i) * D + 2 * c);
    const u32x4 sw = *(const u32x4*)(shift_k + 8 * c);
    __builtin_amdgcn_sched_barrier(0);
    const u32x4 pa = *(const u32x4*)(pf0 + (size_t)(b * 640 + tid) * 8);
    const u32x4 pb = *(const u32x4*)(pf1 + (size_t)(b * 640 + tid) * 8);
    u32x4 pc0 = {0u, 0u, 0u, 0u}, pc1 = pc0;
    if (b < 8) {
        u32 alo, ahi, elo, ehi;
        asm volatile(
            "s_getpc_b64 s[20:21]\n\t"
            "s_add_u32 s20, s20, k1_conv@rel32@lo+4\n\t"
            "s_addc_u32 s21, s21, k1_conv@rel32@hi+12\n\t"
            "s_getpc_b64 s[22:23]\n\t"
            "s_add_u32 s22, s22, k9_end_marker@rel32@lo+4\n\t"
            "s_addc_u32 s23, s23, k9_end_marker@rel32@hi+12\n\t"
            "s_mov_b32 %0, s20\n\ts_mov_b32 %1, s21\n\ts_mov_b32 %2, s22\n\ts_mov_b32 %3, s23"
            : "=s"(alo), "=s"(ahi), "=s"(elo), "=s"(ehi)::"s20", "s21", "s22", "s23", "scc");
        const char* cb = (const char*)(((unsigned long long)ahi << 32) | alo);
        const char* ce = (const char*)(((unsigned long long)ehi << 32) | elo);
        const long long len = ce - cb;
        const long long o0 = (long long)tid * 16, o1 = (long long)(tid + 640) * 16;
        if (len > 0 && o0 + 16 <= len) pc0 = *(const u32x4*)(cb + o0);
        if (len > 0 && o1 + 16 <= len) pc1 = *(const u32x4*)(cb + o1);
    } else if (b < 16) {
        u32 alo, ahi, elo, ehi;
        asm volatile(
            "s_getpc_b64 s[20:21]\n\t"
            "s_add_u32 s20, s20, k1_conv.kd@rel32@lo+4\n\t"
            "s_addc_u32 s21, s21, k1_conv.kd@rel32@hi+12\n\t"
            "s_getpc_b64 s[22:23]\n\t"
            "s_add_u32 s22, s22, k9_end_marker.kd@rel32@lo+4\n\t"
            "s_addc_u32 s23, s23, k9_end_marker.kd@rel32@hi+12\n\t"
            "s_mov_b32 %0, s20\n\ts_mov_b32 %1, s21\n\ts_mov_b32 %2, s22\n\ts_mov_b32 %3, s23"
            : "=s"(alo), "=s"(ahi), "=s"(elo), "=s"(ehi)::"s20", "s21", "s22", "s23", "scc");
        const char* cb = (const char*)(((unsigned long long)ahi << 32) | alo);
        const char* ce = (const char*)(((unsigned long long)ehi << 32) | elo);
        const long long len = ce - cb;
        const long long o0 = (long long)tid * 16;
        if (len > 0 && len <= 4096 && o0 + 16 <= len) pc0 = *(const u32x4*)(cb + o0);
    }
    __builtin_amdgcn_sched_barrier(0);
    if (t0 == 0) { kk[0] = 0u; kk[1] = 0u; kk[2] = 0u; }
    const float a0 = lo2f(sw.x), a1 = hi2f(sw.x), a2 = lo2f(sw.y), a3 = hi2f(sw.y);
    const float b0 = lo2f(sw.z), b1 = hi2f(sw.z), b2 = lo2f(sw.w), b3 = hi2f(sw.w);
    float ua[4], ub[4];
#pragma unroll
    for (int i = 0; i < 4; ++i) {
        u32 p0 = pk2(lo2f(kk[i]) * a3, hi2f(kk[i]) * b3);
        u32 p1 = pk2(lo2f(kk[i + 1]) * a2, hi2f(kk[i + 1]) * b2);
        u32 p2 = pk2(lo2f(kk[i + 2]) * a1, hi2f(kk[i + 2]) * b1);
        u32 p3 = pk2(lo2f(kk[i + 3]) * a0, hi2f(kk[i + 3]) * b0);
        u32 s = pk2(lo2f(p0) + lo2f(p1), hi2f(p0) + hi2f(p1));
        s = pk2(lo2f(s) + lo2f(p2), hi2f(s) + hi2f(p2));
        s = pk2(lo2f(s) + lo2f(p3), hi2f(s) + hi2f(p3));
        ua[i] = lo2f(s) * lo2f(vv[i]);
        ub[i] = hi2f(s) * hi2f(vv[i]);
    }
    u32x2 oa, ob;
    oa.x = pk2(ua[3], ua[2]); oa.y = pk2(ua[1], ua[0]);
    ob.x = pk2(ub[3], ub[2]); ob.y = pk2(ub[1], ub[0]);
    *(u32x2*)(tile + (2 * cp) * 72 + ((rg * 4) ^ 4)) = oa;
    *(u32x2*)(tile + (2 * cp + 1) * 72 + ((rg * 4) ^ 4)) = ob;
    __syncthreads();
    {
        const int ch = tid >> 3, pc = tid & 7;
        const u32x4 o = *(const u32x4*)(tile + ch * 72 + pc * 8);
        st16(uT + (size_t)(ct * 80 + ch) * S + rt * 64 + pc * 8, o);
    }
    KEEP4(pa);
    KEEP4(pb);
    KEEP4(pc0);
    KEEP4(pc1);
}

constexpr int UPAD = 256;
constexpr int KCS = 2080;
extern "C" __global__ __launch_bounds__(256, 3) void k1_conv(const u16* __restrict__ uT, const u16* __restrict__ ssm_k,
                                               const u16* __restrict__ ssm_D, u16* __restrict__ y2T,
                                               const u16* __restrict__ w_gate, const u16* __restrict__ w_up,
                                               const u16* __restrict__ w_down, u16* __restrict__ wgF,
                                               u16* __restrict__ wuF, u16* __restrict__ wdF) {
    __shared__ __attribute__((aligned(16))) u16 lds[UPAD + S + 12288 + 3 * 2048];
    u16* uL = lds;
    u16* kL = lds + UPAD + S;
    f32x4* part = (f32x4*)(lds + UPAD + S);
    const int tid = threadIdx.x;
    const int w = __builtin_amdgcn_readfirstlane(tid >> 6), lane = tid & 63;
    const int d = blockIdx.x;
    const u16* urow = uT + (size_t)d * S;
    const u16* krow = ssm_k + (size_t)d * S;
    const u32x4 z4 = {0u, 0u, 0u, 0u};
    u32x4 ur, kd;
    u32x2 ke;
    const int T = w * D + d;
    const int tm = T / 540, trem = T - tm * 540;
    const int tks = (tm < 2) ? trem / 27 : trem / 10;
    const int tcg = (tm < 2) ? trem - tks * 27 : trem - tks * 10;
    u16* tl = lds + UPAD + S + 12288 + w * 2048;
    ur = *(const u32x4*)(urow + tid * 8);
    kd = *(const u32x4*)(krow + tid * 8);
    ke = *(const u32x2*)(krow + (tid < 255 ? tid * 8 + 8 : 0));
    const u32 dpair = ((const u32*)ssm_D)[d >> 1];
    const float Dv = (d & 1) ? hi2f(dpair) : lo2f(dpair);
    if (tid == 255) ke = (u32x2){0u, 0u};
    *(u32x4*)(uL + UPAD + tid * 8) = ur;
    if (tid < 32) *(u32x4*)(uL + tid * 8) = z4;
    {
        u32 d0 = kd.x, d1 = kd.y, d2 = kd.z, d3 = kd.w, d4 = ke.x, d5 = ke.y;
        u32 s0 = __builtin_amdgcn_alignbit(d1, d0, 16);
        u32 s1 = __builtin_amdgcn_alignbit(d2, d1, 16);
        u32 s2 = __builtin_amdgcn_alignbit(d3, d2, 16);
        u32 s3 = __builtin_amdgcn_alignbit(d4, d3, 16);
        u32 s4 = __builtin_amdgcn_alignbit(d5, d4, 16);
        u32x4 c0 = {d0, d1, d2, d3}, c1 = {s0, s1, s2, s3}, c2 = {d1, d2, d3, d4}, c3 = {s1, s2, s3, s4};
        const int Y = tid + 2;
        *(u32x4*)(kL + 0 * KCS + Y * 8) = c0;
        *(u32x4*)(kL + 1 * KCS + Y * 8) = c1;
        *(u32x4*)(kL + 2 * KCS + Y * 8) = c2;
        *(u32x4*)(kL + 3 * KCS + Y * 8) = c3;
        if (tid == 0) {
            u32x4 e1 = {0u, 0u, 0u, d0 << 16}, e2 = {0u, 0u, 0u, d0}, e3 = {0u, 0u, d0 << 16, s0};
            *(u32x4*)(kL + 0 * KCS + 0) = z4; *(u32x4*)(kL + 0 * KCS + 8) = z4;
            *(u32x4*)(kL + 1 * KCS + 0) = z4; *(u32x4*)(kL + 1 * KCS + 8) = e1;
            *(u32x4*)(kL + 2 * KCS + 0) = z4; *(u32x4*)(kL + 2 * KCS + 8) = e2;
            *(u32x4*)(kL + 3 * KCS + 0) = z4; *(u32x4*)(kL + 3 * KCS + 8) = e3;
        }
    }
    __syncthreads();
    if (T < 1620) {
        const u16* src = (tm < 2) ? ((tm ? w_up : w_gate) + (size_t)(tks * 32) * F + tcg * 64)
                                  : (w_down + (size_t)(tks * 32) * D + tcg * 64);
        const int ldm = (tm < 2) ? F : D;
        const u16* g0 = src + (size_t)(lane >> 3) * ldm + (lane & 7) * 8;
        const u16* g1 = g0 + (size_t)8 * ldm;
        const u16* g2 = g0 + (size_t)16 * ldm;
        const u16* g3 = g0 + (size_t)24 * ldm;
        const u32 l0 = __builtin_amdgcn_readfirstlane(lds_addr(tl));
        u32 keep;
        asm volatile(
            "s_mov_b32 %0, m0\n\t"
            "s_mov_b32 m0, %5\n\ts_nop 0\n\tglobal_load_lds_dwordx4 %1, off\n\t"
            "s_add_u32 m0, %5, 0x400\n\ts_nop 0\n\tglobal_load_lds_dwordx4 %2, off\n\t"
            "s_add_u32 m0, %5, 0x800\n\ts_nop 0\n\tglobal_load_lds_dwordx4 %3, off\n\t"
            "s_add_u32 m0, %5, 0xc00\n\ts_nop 0\n\tglobal_load_lds_dwordx4 %4, off\n\t"
            "s_mov_b32 m0, %0"
            : "=&s"(keep)
            : "v"(g0), "v"(g1), "v"(g2), "v"(g3), "s"(l0)
            : "memory", "scc");
    }
    const int li = lane & 15, g = lane >> 4;
    const u16* aBase = kL + ((li + 1) & 3) * KCS + ((li + 1) & ~3) + 8 * g + 64 * w;
    const u16* bBase = uL + UPAD + 16 * li + 8 - 8 * g - 64 * w;
    f32x4 acc[8];
#pragma unroll
    for (int c = 0; c < 8; ++c) acc[c] = (f32x4){0.f, 0.f, 0.f, 0.f};
    const u32 aAddr = lds_addr(aBase);
    const u32 bAddr = lds_addr(bBase - 32);
    s16x4 alo[2][8], ahi[2][8];
    bf16x8 bf[2][8];
#define LOAD_FRAGS(r)                                                                                   \
    _Pragma("unroll") for (int n = 0; n < 8; ++n) {                                                     \
        asm volatile("ds_read_b64 %0, %1 offset:%2" : "=v"(alo[r][n]) : "v"(aAddr), "i"(64 * (8 * n + r))); \
        asm volatile("ds_read_b64 %0, %1 offset:%2" : "=v"(ahi[r][n]) : "v"(aAddr), "i"(64 * (8 * n + r) + 8)); \
        asm volatile("ds_read_b128 %0, %1 offset:%2" : "=v"(bf[r][n]) : "v"(bAddr), "i"(64 * (8 * n - r) + 64)); \
    }
#define MFMA_ALL(r)                                                                                     \
    _Pragma("unroll") for (int sum = 0; sum < 8; ++sum) _Pragma("unroll") for (int n = 0; n <= sum; ++n) \
        acc[sum] = __builtin_amdgcn_mfma_f32_16x16x32_bf16(                                             \
            __builtin_shufflevector(alo[r][n], ahi[r][n], 0, 1, 2, 3, 4, 5, 6, 7), bf[r][sum - n], acc[sum], 0, 0, 0);
    LOAD_FRAGS(0)
    asm volatile("s_waitcnt lgkmcnt(0)" ::: "memory");
    __builtin_amdgcn_sched_barrier(0);
    LOAD_FRAGS(1)
    __builtin_amdgcn_sched_barrier(0);
    MFMA_ALL(0)
    __builtin_amdgcn_sched_barrier(0);
    asm volatile("s_waitcnt lgkmcnt(0)" ::: "memory");
    __builtin_amdgcn_sched_barrier(0);
    MFMA_ALL(1)
#undef LOAD_FRAGS
#undef MFMA_ALL
    __syncthreads();
#pragma unroll
    for (int c = 0; c < 8; ++c) {
        const int o = c >> 1;
        if (o != w) part[((o * 3 + ((w - o - 1) & 3)) * 2 + (c & 1)) * 64 + lane] = acc[c];
    }
    __syncthreads();
    if (T < 1620) {
        asm volatile("s_waitcnt vmcnt(0)" ::: "memory");
#pragma unroll
        for (int j = 0; j < 4; ++j) {
            const u16* rp = tl + (8 * g + (li >> 2)) * 64 + 16 * j + 4 * (li & 3);
            s16x4 b0 = tr_read(rp), b1 = tr_read(rp + 4 * 64);
            bf16x8 fr = __builtin_shufflevector(b0, b1, 0, 1, 2, 3, 4, 5, 6, 7);
            u16* dst;
            if (tm < 2) dst = (tm ? wuF : wgF) + ((size_t)((tcg * 4 + j) * 20 + tks) * 64 + lane) * 8;
            else dst = wdF + ((size_t)((tcg * 8 + 2 * j + (li >> 3)) * 54 + tks) * 32 + g * 8 + (li & 7)) * 8;
            asm volatile("global_store_dwordx4 %0, %1, off sc1\n\ts_nop 1" ::"v"(dst), "v"(fr) : "memory");
        }
    }
    u16* yrow = y2T + (size_t)d * S;
    u32x2 ov[2];
#pragma unroll
    for (int j = 0; j < 2; ++j) {
        const int c = 2 * w + j;
        f32x4 own = acc[j];
#pragma unroll
        for (int q = 1; q < 4; ++q) own = (w == q) ? acc[2 * q + j] : own;
        f32x4 a0 = part[((w * 3 + 0) * 2 + j) * 64 + lane], a1 = part[((w * 3 + 1) * 2 + j) * 64 + lane];
        f32x4 a2 = part[((w * 3 + 2) * 2 + j) * 64 + lane];
        f32x4 a = (a0 + a1) + (a2 + own);
        const int tb = 256 * c + 16 * li + 8 * (g >> 1);
        u32x2 uu = *(const u32x2*)(uL + UPAD + tb + 4 * (1 - (g & 1)));
        float u3 = lo2f(uu.x), u2 = hi2f(uu.x), u1 = lo2f(uu.y), u0 = hi2f(uu.y);
        u32 y01 = pk2(a[0], a[1]);
        u32 y23 = pk2(a[2], a[3]);
        u32 w01 = pk2(u0 * Dv, u1 * Dv);
        u32 w23 = pk2(u2 * Dv, u3 * Dv);
        ov[j].x = pk2(lo2f(y01) + lo2f(w01), hi2f(y01) + hi2f(w01));
        ov[j].y = pk2(lo2f(y23) + lo2f(w23), hi2f(y23) + hi2f(w23));
    }
    {
        auto rx = __builtin_amdgcn_permlane16_swap(ov[0].x, ov[1].x, false, false);
        auto ry = __builtin_amdgcn_permlane16_swap(ov[0].y, ov[1].y, false, false);
        u32x4 o = {rx[0], ry[0], rx[1], ry[1]};
        st16(yrow + 256 * (2 * w + (g & 1)) + 16 * li + 8 * (g >> 1), o);
    }
    asm volatile("s_waitcnt vmcnt(0)" ::: "memory");
}

__device__ __forceinline__ void unpack10(const u32x4& a, u32 b, float* f) {
    f[0] = lo2f(a.x); f[1] = hi2f(a.x); f[2] = lo2f(a.y); f[3] = hi2f(a.y);
    f[4] = lo2f(a.z); f[5] = hi2f(a.z); f[6] = lo2f(a.w); f[7] = hi2f(a.w);
    f[8] = lo2f(b); f[9] = hi2f(b);
}
__device__ __forceinline__ void ln_stats(const float* x, float& m_bf, float& s_bf) {
    float s = ((x[0] + x[1]) + (x[2] + x[3])) + ((x[4] + x[5]) + (x[6] + x[7])) + (x[8] + x[9]);
    s = wave_sum(s);
    const float mean = s * (1.0f / D);
    float q0 = 0.f, q1 = 0.f;
#pragma unroll
    for (int i = 0; i < 10; i += 2) {
        float d0 = x[i] - mean, d1 = x[i + 1] - mean;
        q0 += d0 * d0;
        q1 += d1 * d1;
    }
    float q = wave_sum(q0 + q1);
    const float var = q * (1.0f / D);
    m_bf = rbf(mean);
    float vb = rbf(var);
    float ve = rbf(vb + 1e-5f);
    s_bf = rbf(sqrtf(ve));
}

__global__ __launch_bounds__(512) void k2_row(const u16* __restrict__ q, const u16* __restrict__ y2T,
                                              const u16* __restrict__ w_r, u16* __restrict__ out,
                                              u16* __restrict__ hbuf, float* __restrict__ stats,
                                              u32* __restrict__ keys) {
    __shared__ __attribute__((aligned(16))) u32 yt[8][320];
    const int b = blockIdx.x;
    const int tb = (b & 7) * 32 + (b >> 3);
    const int t0 = tb * 8;
    const int tid = threadIdx.x;
    const int w = tid >> 6, lane = tid & 63;
    const int t = t0 + w;
    const u32x4 xa = *(const u32x4*)(q + (size_t)t * D + 8 * lane);
    const u32 xb = *(const u32*)(q + (size_t)t * D + 512 + 2 * lane);
    u32x4 ya = {0u, 0u, 0u, 0u}, yb = {0u, 0u, 0u, 0u};
    if (tid < 320) {
        ya = *(const u32x4*)(y2T + (size_t)(2 * tid) * S + t0);
        yb = *(const u32x4*)(y2T + (size_t)(2 * tid + 1) * S + t0);
    }
    const u32x4 wa = *(const u32x4*)(w_r + 8 * lane);
    const u32 wb = *(const u32*)(w_r + 512 + 2 * lane);
    float x[10];
    unpack10(xa, xb, x);
    float m1, s1;
    ln_stats(x, m1, s1);
    const float r1 = 1.0f / s1;
    if (tid < 320) {
        yt[0][tid] = __builtin_amdgcn_perm(yb.x, ya.x, 0x05040100);
        yt[1][tid] = __builtin_amdgcn_perm(yb.x, ya.x, 0x07060302);
        yt[2][tid] = __builtin_amdgcn_perm(yb.y, ya.y, 0x05040100);
        yt[3][tid] = __builtin_amdgcn_perm(yb.y, ya.y, 0x07060302);
        yt[4][tid] = __builtin_amdgcn_perm(yb.z, ya.z, 0x05040100);
        yt[5][tid] = __builtin_amdgcn_perm(yb.z, ya.z, 0x07060302);
        yt[6][tid] = __builtin_amdgcn_perm(yb.w, ya.w, 0x05040100);
        yt[7][tid] = __builtin_amdgcn_perm(yb.w, ya.w, 0x07060302);
    }
    u32 l1p[5];
#pragma unroll
    for (int i = 0; i < 10; i += 2) {
        u32 dd = pk2(x[i] - m1, x[i + 1] - m1);
        l1p[i >> 1] = pk2(lo2f(dd) * r1, hi2f(dd) * r1);
    }
    __syncthreads();
    const u32x4 y4 = *(const u32x4*)(&yt[w][4 * lane]);
    const u32 y1 = yt[w][256 + lane];
    float y[10];
    unpack10(y4, y1, y);
    float h[10];
    u32 hp[5], op[5];
#pragma unroll
    for (int i = 0; i < 10; i += 2) {
        u32 l1 = l1p[i >> 1];
        u32 a = pk2(lo2f(l1) * y[i], hi2f(l1) * y[i + 1]);
        u32 hh = pk2(x[i] + lo2f(a), x[i + 1] + hi2f(a));
        hp[i >> 1] = hh;
        h[i] = lo2f(hh);
        h[i + 1] = hi2f(hh);
    }
    st16(hbuf + (size_t)t * D + 8 * lane, (u32x4){hp[0], hp[1], hp[2], hp[3]});
    {
        u32x4 tq = {(u32)__builtin_amdgcn_mov_dpp((int)hp[4], 0x00, 0xf, 0xf, true), (u32)__builtin_amdgcn_mov_dpp((int)hp[4], 0x55, 0xf, 0xf, true),
                    (u32)__builtin_amdgcn_mov_dpp((int)hp[4], 0xaa, 0xf, 0xf, true), (u32)__builtin_amdgcn_mov_dpp((int)hp[4], 0xff, 0xf, 0xf, true)};
        if ((lane & 3) == 0) st16(hbuf + (size_t)t * D + 512 + 2 * lane, tq);
    }
    float m2, s2;
    ln_stats(h, m2, s2);
    const float r2 = 1.0f / s2;
    float wr[10];
    unpack10(wa, wb, wr);
    float racc0 = 0.f, racc1 = 0.f;
#pragma unroll
    for (int i = 0; i < 10; i += 2) {
        u32 dd = pk2(h[i] - m2, h[i + 1] - m2);
        u32 l2 = pk2(lo2f(dd) * r2, hi2f(dd) * r2);
        racc0 += lo2f(l2) * wr[i];
        racc1 += hi2f(l2) * wr[i + 1];
        op[i >> 1] = pk2(h[i] + lo2f(l2), h[i + 1] + hi2f(l2));
    }
    st16(out + (size_t)t * D + 8 * lane, (u32x4){op[0], op[1], op[2], op[3]});
    {
        u32x4 tq = {(u32)__builtin_amdgcn_mov_dpp((int)op[4], 0x00, 0xf, 0xf, true), (u32)__builtin_amdgcn_mov_dpp((int)op[4], 0x55, 0xf, 0xf, true),
                    (u32)__builtin_amdgcn_mov_dpp((int)op[4], 0xaa, 0xf, 0xf, true), (u32)__builtin_amdgcn_mov_dpp((int)op[4], 0xff, 0xf, 0xf, true)};
        if ((lane & 3) == 0) st16(out + (size_t)t * D + 512 + 2 * lane, tq);
    }
    const float racc = wave_sum(racc0 + racc1);
    if (lane == 0) {
        u32 rb = pk2(racc, 0.f) & 0xffffu;
        u32 ord = (rb & 0x8000u) ? (rb ^ 0xffffu) : (rb ^ 0x8000u);
        keys[t] = (ord << 16) | (u32)(0xffff - t);
        *(f32x2*)(stats + 2 * t) = (f32x2){m2, r2};
    }
}

__device__ __forceinline__ float key_r(u32 key) {
    u32 ord = key >> 16;
    u32 rb = (ord & 0x8000u) ? (ord ^ 0x8000u) : (ord ^ 0xffffu);
    return __uint_as_float(rb << 16);
}

constexpr int XS = 648;
__global__ __launch_bounds__(512) void k3_gateup(const u32* __restrict__ keys, const u16* __restrict__ hbuf,
                                                 const float* __restrict__ stats, const u16* __restrict__ wgF,
                                                 const u16* __restrict__ wuF, u16* __restrict__ gu,
                                                 u32* __restrict__ selkeys, u16* __restrict__ selh,
                                                 u16* __restrict__ sell2) {
    __shared__ __attribute__((aligned(16))) u16 xs[8 * XS];
    __shared__ __attribute__((aligned(16))) float red[8][2][8][16];
    __shared__ u32 cand[64];
    const int b = blockIdx.x;
    const int slice = b;
    const int f0 = slice * 16;
    const int tid = threadIdx.x, lane = tid & 63;
    const int w = __builtin_amdgcn_readfirstlane(tid >> 6);
    u32 a[4];
    {
        u32x4 k0 = *(const u32x4*)(keys + tid * 4);
        a[0] = k0.x; a[1] = k0.y; a[2] = k0.z; a[3] = k0.w;
    }
    bf16x8 bg[3], bu[3];
#pragma unroll
    for (int i = 0; i < 3; ++i) {
        const int ks = (w + 8 * i < 20) ? (w + 8 * i) : w;
        bg[i] = *(const bf16x8*)(wgF + ((size_t)(slice * 20 + ks) * 64 + lane) * 8);
        bu[i] = *(const bf16x8*)(wuF + ((size_t)(slice * 20 + ks) * 64 + lane) * 8);
    }
#define CE(i, j) { u32 hi_ = a[i] > a[j] ? a[i] : a[j]; u32 lo_ = a[i] > a[j] ? a[j] : a[i]; a[i] = hi_; a[j] = lo_; }
    CE(0, 1) CE(2, 3) CE(0, 2) CE(1, 3) CE(1, 2)
#undef CE
    u32 mine = 0;
#pragma unroll
    for (int rd = 0; rd < 8; ++rd) {
        u32 m = wave_max_u32(a[0]);
        bool win = (a[0] == m);
        a[0] = win ? a[1] : a[0];
        a[1] = win ? a[2] : a[1];
        a[2] = win ? a[3] : a[2];
        a[3] = win ? 0u : a[3];
        if (lane == rd) mine = m;
    }
    if (lane < 8) cand[w * 8 + lane] = mine;
    __syncthreads();
    {
        u32 c = cand[lane];
        u32 key = 0;
        for (int rd = 0; rd <= w; ++rd) {
            key = wave_max_u32(c);
            c = (c == key) ? 0u : c;
        }
        const int ta = 0xffff - (int)(key & 0xffffu);
        const u32x4 ha = *(const u32x4*)(hbuf + (size_t)ta * D + 8 * lane);
        const u32 hb = *(const u32*)(hbuf + (size_t)ta * D + 512 + 2 * lane);
        const f32x2 st0 = *(const f32x2*)(stats + 2 * ta);
        const float m2 = st0.x, r2 = st0.y;
        float h[10];
        unpack10(ha, hb, h);
        u32 l2[5];
#pragma unroll
        for (int i = 0; i < 10; i += 2) {
            u32 dd = pk2(h[i] - m2, h[i + 1] - m2);
            l2[i >> 1] = pk2(lo2f(dd) * r2, hi2f(dd) * r2);
        }
        *(u32x4*)(xs + w * XS + 8 * lane) = (u32x4){l2[0], l2[1], l2[2], l2[3]};
        *(u32*)(xs + w * XS + 512 + 2 * lane) = l2[4];
        if (b == 0) {
            *(u32x4*)(selh + w * D + 8 * lane) = ha;
            *(u32*)(selh + w * D + 512 + 2 * lane) = hb;
            *(u32x4*)(sell2 + w * D + 8 * lane) = (u32x4){l2[0], l2[1], l2[2], l2[3]};
            *(u32*)(sell2 + w * D + 512 + 2 * lane) = l2[4];
            if (lane == 0) selkeys[w] = key;
        }
    }
    __syncthreads();
    {
        const int li = lane & 15, g = lane >> 4;
        f32x4 ag = {0.f, 0.f, 0.f, 0.f}, au = {0.f, 0.f, 0.f, 0.f};
#pragma unroll
        for (int s = 0; s < 2; ++s) {
            const int k0 = (w + 8 * s) * 32;
            bf16x8 af = *(const bf16x8*)(xs + (li & 7) * XS + k0 + 8 * g);
            ag = __builtin_amdgcn_mfma_f32_16x16x32_bf16(af, bg[s], ag, 0, 0, 0);
            au = __builtin_amdgcn_mfma_f32_16x16x32_bf16(af, bu[s], au, 0, 0, 0);
        }
        {
            const int k0 = ((w < 4) ? (w + 16) : w) * 32;
            bf16x8 af = *(const bf16x8*)(xs + (li & 7) * XS + k0 + 8 * g);
            f32x4 ag2 = __builtin_amdgcn_mfma_f32_16x16x32_bf16(af, bg[2], ag, 0, 0, 0);
            f32x4 au2 = __builtin_amdgcn_mfma_f32_16x16x32_bf16(af, bu[2], au, 0, 0, 0);
            if (w < 4) { ag = ag2; au = au2; }
        }
        if (g < 2) {
#pragma unroll
            for (int rr = 0; rr < 4; ++rr) {
                red[w][0][4 * g + rr][li] = ag[rr];
                red[w][1][4 * g + rr][li] = au[rr];
            }
        }
    }
    __syncthreads();
    if (tid < 128) {
        const int tok = tid >> 4, col = tid & 15;
        float gs = ((red[0][0][tok][col] + red[1][0][tok][col]) + (red[2][0][tok][col] + red[3][0][tok][col])) +
                   ((red[4][0][tok][col] + red[5][0][tok][col]) + (red[6][0][tok][col] + red[7][0][tok][col]));
        float us = ((red[0][1][tok][col] + red[1][1][tok][col]) + (red[2][1][tok][col] + red[3][1][tok][col])) +
                   ((red[4][1][tok][col] + red[5][1][tok][col]) + (red[6][1][tok][col] + red[7][1][tok][col]));
        float gb = rbf(gs), ub = rbf(us);
        float sg = rbf(gb / (1.0f + __expf(-gb)));
        u32 o = pk2(sg * ub, 0.f);
        const int f = f0 + col;
        gu[((f >> 5) * 4 + ((f >> 3) & 3)) * 64 + tok * 8 + (f & 7)] = (u16)(o & 0xffffu);
    }
}

__global__ __launch_bounds__(512) void k4_down(const u16* __restrict__ gu, const u16* __restrict__ wdF,
                                               const u32* __restrict__ selkeys, const u16* __restrict__ selh,
                                               const u16* __restrict__ sell2, u16* __restrict__ out) {
    __shared__ __attribute__((aligned(16))) float red[8][8][16];
    const int slice = blockIdx.x;
    const int d0 = slice * 8;
    const int tid = threadIdx.x, lane = tid & 63;
    const int w = __builtin_amdgcn_readfirstlane(tid >> 6);
    const int li = lane & 15, g = lane >> 4;
    bf16x8 af[7], bf[7];
    {
        const u16* pa = gu + (w * 4 + g) * 64 + (li & 7) * 8;
        const u16* pb = wdF + ((size_t)(slice * 54 + w) * 32 + g * 8 + (li & 7)) * 8;
#pragma unroll
        for (int j = 0; j < 7; ++j) {
            const int jj = (j == 6 && w >= 6) ? 5 : j;
            af[j] = *(const bf16x8*)(pa + jj * 8 * 256);
            bf[j] = *(const bf16x8*)(pb + jj * 8 * 256);
        }
    }
    const int tok_ = (tid >> 3) & 7, col_ = tid & 7;
    const u32 key = selkeys[tok_];
    const float hv = __uint_as_float(((u32)selh[tok_ * D + d0 + col_]) << 16);
    const float l2v = __uint_as_float(((u32)sell2[tok_ * D + d0 + col_]) << 16);
    __builtin_amdgcn_sched_barrier(0);
    {
        f32x4 acc = {0.f, 0.f, 0.f, 0.f};
#pragma unroll
        for (int j = 0; j < 6; ++j) acc = __builtin_amdgcn_mfma_f32_16x16x32_bf16(af[j], bf[j], acc, 0, 0, 0);
        f32x4 acc2 = __builtin_amdgcn_mfma_f32_16x16x32_bf16(af[6], bf[6], acc, 0, 0, 0);
        if (w < 6) acc = acc2;
        if (g < 2) {
#pragma unroll
            for (int rr = 0; rr < 4; ++rr) red[w][4 * g + rr][li] = acc[rr];
        }
    }
    __syncthreads();
    if (tid < 64) {
        const int tok = tid >> 3, col = tid & 7;
        const int trow = 0xffff - (int)(key & 0xffffu);
        const float rv = key_r(key);
        const float rs = rbf(1.0f / (1.0f + __expf(-rv)));
        float s = ((red[0][tok][col] + red[1][tok][col]) + (red[2][tok][col] + red[3][tok][col])) +
                  ((red[4][tok][col] + red[5][tok][col]) + (red[6][tok][col] + red[7][tok][col]));
        float y = rbf(s);
        y = rbf(y * rs);
        float o1 = rbf(l2v + y);
        u32 o = pk2(hv + o1, 0.f);
        out[(size_t)trow * D + d0 + col] = (u16)(o & 0xffffu);
    }
}

extern "C" __global__ void k9_end_marker(int* p) { if (p == nullptr) *p = 0; }

extern "C" void kernel_launch(void* const* d_in, const int* in_sizes, int n_in, void* d_out, int out_size,
                              void* d_ws, size_t ws_size, hipStream_t stream) {
    const u16* q = (const u16*)d_in[0];
    const u16* k = (const u16*)d_in[1];
    const u16* v = (const u16*)d_in[2];
    const u16* shift_k = (const u16*)d_in[3];
    const u16* ssm_k = (const u16*)d_in[4];
    const u16* ssm_D = (const u16*)d_in[5];
    const u16* w_r = (const u16*)d_in[7];
    const u16* w_gate = (const u16*)d_in[8];
    const u16* w_up = (const u16*)d_in[9];
    const u16* w_down = (const u16*)d_in[10];
    u16* out = (u16*)d_out;
    char* ws = (char*)d_ws;
    const size_t TS = (size_t)S * D * 2;
    u16* uT = (u16*)(ws);
    u16* y2T = (u16*)(ws + TS);
    u16* hbuf = (u16*)(ws);
    char* sm = ws + 2 * TS;
    float* stats = (float*)(sm);
    u32* keys = (u32*)(sm + 16384);
    u32* selkeys = (u32*)(sm + 24576);
    u16* selh = (u16*)(sm + 24576 + 256);
    u16* sell2 = (u16*)(sm + 24576 + 256 + 10240);
    u16* gu = (u16*)(sm + 24576 + 256 + 20480);
    const size_t WS_ = (size_t)D * F * 2;
    u16* wgF = (u16*)(sm + 131072);
    u16* wuF = (u16*)(sm + 131072 + WS_);
    u16* wdF = (u16*)(sm + 131072 + 2 * WS_);
    k0_kv<<<256, 640, 0, stream>>>(k, v, shift_k, uT, ssm_k, q);
    k1_conv<<<D, 256, 0, stream>>>(uT, ssm_k, ssm_D, y2T, w_gate, w_up, w_down, wgF, wuF, wdF);
    k2_row<<<256, 512, 0, stream>>>(q, y2T, w_r, out, hbuf, stats, keys);
    k3_gateup<<<108, 512, 0, stream>>>(keys, hbuf, stats, wgF, wuF, gu, selkeys, selh, sell2);
    k4_down<<<80, 512, 0, stream>>>(gu, wdF, selkeys, selh, sell2, out);
}
```

```cpp
#include <hip/hip_runtime.h>
#include <hip/hip_bf16.h>

typedef unsigned short u16;
typedef unsigned int u32;
typedef __attribute__((ext_vector_type(8))) short bf16x8;
typedef __attribute__((ext_vector_type(4))) short s16x4;
typedef __attribute__((ext_vector_type(4))) float f32x4;
typedef __attribute__((ext_vector_type(2))) float f32x2;
typedef __attribute__((ext_vector_type(2))) __bf16 bfx2;
typedef __attribute__((ext_vector_type(4))) unsigned u32x4;
typedef __attribute__((ext_vector_type(2))) unsigned u32x2;

constexpr int S = 2048, D = 640, F = 1728, NSEL = 8;


__device__ __forceinline__ u32 pk2(float a, float b) {
    f32x2 v = {a, b};
    bfx2 r = __builtin_convertvector(v, bfx2);
    return __builtin_bit_cast(u32, r);
}
__device__ __forceinline__ float lo2f(u32 p) { return __uint_as_float(p << 16); }
__device__ __forceinline__ float hi2f(u32 p) { return __uint_as_float(p & 0xffff0000u); }
__device__ __forceinline__ float rbf(float a) { return lo2f(pk2(a, 0.f)); }

template <int CTRL, int RM> __device__ __forceinline__ int dpp_i(int v) {
    return __builtin_amdgcn_update_dpp(0, v, CTRL, RM, 0xf, true);
}
__device__ __forceinline__ float wave_sum(float v) {
    v += __int_as_float(dpp_i<0x111, 0xf>(__float_as_int(v)));
    v += __int_as_float(dpp_i<0x112, 0xf>(__float_as_int(v)));
    v += __int_as_float(dpp_i<0x114, 0xf>(__float_as_int(v)));
    v += __int_as_float(dpp_i<0x118, 0xf>(__float_as_int(v)));
    v += __int_as_float(dpp_i<0x142, 0xa>(__float_as_int(v)));
    v += __int_as_float(dpp_i<0x143, 0xc>(__float_as_int(v)));
    return __int_as_float(__builtin_amdgcn_readlane(__float_as_int(v), 63));
}
__device__ __forceinline__ u32 umax_(u32 a, u32 b) { return a > b ? a : b; }
__device__ __forceinline__ u32 wave_max_u32(u32 v) {
    v = umax_(v, (u32)dpp_i<0x111, 0xf>((int)v));
    v = umax_(v, (u32)dpp_i<0x112, 0xf>((int)v));
    v = umax_(v, (u32)dpp_i<0x114, 0xf>((int)v));
    v = umax_(v, (u32)dpp_i<0x118, 0xf>((int)v));
    v = umax_(v, (u32)dpp_i<0x142, 0xa>((int)v));
    v = umax_(v, (u32)dpp_i<0x143, 0xc>((int)v));
    return (u32)__builtin_amdgcn_readlane((int)v, 63);
}
#define KEEP4(val_) asm volatile("" ::"v"((val_).x), "v"((val_).y), "v"((val_).z), "v"((val_).w))

typedef __attribute__((address_space(3))) s16x4* lds_s16x4_ptr;
__device__ __forceinline__ s16x4 tr_read(const u16* p) {
    return __builtin_amdgcn_ds_read_tr16_b64_v4i16((lds_s16x4_ptr)p);
}
__device__ __forceinline__ u32 lds_addr(const u16* p) {
    return (u32)(size_t)(const __attribute__((address_space(3))) u16*)p;
}
__device__ __forceinline__ void st16(void* p, u32x4 v) { *(u32x4*)p = v; }
__device__ __forceinline__ void wave_lds_fence() {
    __builtin_amdgcn_fence(__ATOMIC_RELEASE, "wavefront");
    __builtin_amdgcn_wave_barrier();
    __builtin_amdgcn_fence(__ATOMIC_ACQUIRE, "wavefront");
}

__global__ __launch_bounds__(640) void k0_kv(const u16* __restrict__ k, const u16* __restrict__ v,
                                             const u16* __restrict__ shift_k, u16* __restrict__ uT,
                                             const u16* __restrict__ pf0, const u16* __restrict__ pf1) {
    __shared__ __attribute__((aligned(16))) u16 tile[80 * 72];
    const int b = blockIdx.x;
    const int ct = b & 7, rt = b >> 3;
    const int tid = threadIdx.x;
    const int rg = tid / 40, cp = tid - rg * 40;
    const int t0 = rt * 64 + rg * 4;
    const int c = ct * 40 + cp;
    u32 kk[7], vv[4];
#pragma unroll
    for (int i = 0; i < 7; ++i) {
        int t = t0 - 3 + i;
        kk[i] = *(const u32*)(k + (size_t)(t < 0 ? 0 : t) * D + 2 * c);
    }
#pragma unroll
    for (int i = 0; i < 4; ++i) vv[i] = *(const u32*)(v + (size_t)(t0 + i) * D + 2 * c);
    const u32x4 sw = *(const u32x4*)(shift_k + 8 * c);
    __builtin_amdgcn_sched_barrier(0);
    const u32x4 pa = *(const u32x4*)(pf0 + (size_t)(b * 640 + tid) * 8);
    const u32x4 pb = *(const u32x4*)(pf1 + (size_t)(b * 640 + tid) * 8);
    __builtin_amdgcn_sched_barrier(0);
    if (t0 == 0) { kk[0] = 0u; kk[1] = 0u; kk[2] = 0u; }
    const float a0 = lo2f(sw.x), a1 = hi2f(sw.x), a2 = lo2f(sw.y), a3 = hi2f(sw.y);
    const float b0 = lo2f(sw.z), b1 = hi2f(sw.z), b2 = lo2f(sw.w), b3 = hi2f(sw.w);
    float ua[4], ub[4];
#pragma unroll
    for (int i = 0; i < 4; ++i) {
        u32 p0 = pk2(lo2f(kk[i]) * a3, hi2f(kk[i]) * b3);
        u32 p1 = pk2(lo2f(kk[i + 1]) * a2, hi2f(kk[i + 1]) * b2);
        u32 p2 = pk2(lo2f(kk[i + 2]) * a1, hi2f(kk[i + 2]) * b1);
        u32 p3 = pk2(lo2f(kk[i + 3]) * a0, hi2f(kk[i + 3]) * b0);
        u32 s = pk2(lo2f(p0) + lo2f(p1), hi2f(p0) + hi2f(p1));
        s = pk2(lo2f(s) + lo2f(p2), hi2f(s) + hi2f(p2));
        s = pk2(lo2f(s) + lo2f(p3), hi2f(s) + hi2f(p3));
        ua[i] = lo2f(s) * lo2f(vv[i]);
        ub[i] = hi2f(s) * hi2f(vv[i]);
    }
    u32x2 oa, ob;
    oa.x = pk2(ua[3], ua[2]); oa.y = pk2(ua[1], ua[0]);
    ob.x = pk2(ub[3], ub[2]); ob.y = pk2(ub[1], ub[0]);
    *(u32x2*)(tile + (2 * cp) * 72 + ((rg * 4) ^ 4)) = oa;
    *(u32x2*)(tile + (2 * cp + 1) * 72 + ((rg * 4) ^ 4)) = ob;
    __syncthreads();
    {
        const int ch = tid >> 3, pc = tid & 7;
        const u32x4 o = *(const u32x4*)(tile + ch * 72 + pc * 8);
        st16(uT + (size_t)(ct * 80 + ch) * S + rt * 64 + pc * 8, o);
    }
    KEEP4(pa);
    KEEP4(pb);
}

constexpr int UPAD = 256;
constexpr int KCS = 2080;
__global__ __launch_bounds__(256, 3) void k1_conv(const u16* __restrict__ uT, const u16* __restrict__ ssm_k,
                                               const u16* __restrict__ ssm_D, u16* __restrict__ y2T,
                                               const u16* __restrict__ w_gate, const u16* __restrict__ w_up,
                                               const u16* __restrict__ w_down, u16* __restrict__ wgF,
                                               u16* __restrict__ wuF, u16* __restrict__ wdF) {
    __shared__ __attribute__((aligned(16))) u16 lds[UPAD + S + 12288 + 3 * 2048];
    u16* uL = lds;
    u16* kL = lds + UPAD + S;
    f32x4* part = (f32x4*)(lds + UPAD + S);
    const int tid = threadIdx.x;
    const int w = __builtin_amdgcn_readfirstlane(tid >> 6), lane = tid & 63;
    const int d = blockIdx.x;
    const u16* urow = uT + (size_t)d * S;
    const u16* krow = ssm_k + (size_t)d * S;
    const u32x4 z4 = {0u, 0u, 0u, 0u};
    u32x4 ur, kd;
    u32x2 ke;
    const int T = w * D + d;
    const int tm = T / 540, trem = T - tm * 540;
    const int tks = (tm < 2) ? trem / 27 : trem / 10;
    const int tcg = (tm < 2) ? trem - tks * 27 : trem - tks * 10;
    u16* tl = lds + UPAD + S + 12288 + w * 2048;
    ur = *(const u32x4*)(urow + tid * 8);
    kd = *(const u32x4*)(krow + tid * 8);
    ke = *(const u32x2*)(krow + (tid < 255 ? tid * 8 + 8 : 0));
    const u32 dpair = ((const u32*)ssm_D)[d >> 1];
    const float Dv = (d & 1) ? hi2f(dpair) : lo2f(dpair);
    if (tid == 255) ke = (u32x2){0u, 0u};
    *(u32x4*)(uL + UPAD + tid * 8) = ur;
    if (tid < 32) *(u32x4*)(uL + tid * 8) = z4;
    {
        u32 d0 = kd.x, d1 = kd.y, d2 = kd.z, d3 = kd.w, d4 = ke.x, d5 = ke.y;
        u32 s0 = __builtin_amdgcn_alignbit(d1, d0, 16);
        u32 s1 = __builtin_amdgcn_alignbit(d2, d1, 16);
        u32 s2 = __builtin_amdgcn_alignbit(d3, d2, 16);
        u32 s3 = __builtin_amdgcn_alignbit(d4, d3, 16);
        u32 s4 = __builtin_amdgcn_alignbit(d5, d4, 16);
        u32x4 c0 = {d0, d1, d2, d3}, c1 = {s0, s1, s2, s3}, c2 = {d1, d2, d3, d4}, c3 = {s1, s2, s3, s4};
        const int Y = tid + 2;
        *(u32x4*)(kL + 0 * KCS + Y * 8) = c0;
        *(u32x4*)(kL + 1 * KCS + Y * 8) = c1;
        *(u32x4*)(kL + 2 * KCS + Y * 8) = c2;
        *(u32x4*)(kL + 3 * KCS + Y * 8) = c3;
        if (tid == 0) {
            u32x4 e1 = {0u, 0u, 0u, d0 << 16}, e2 = {0u, 0u, 0u, d0}, e3 = {0u, 0u, d0 << 16, s0};
            *(u32x4*)(kL + 0 * KCS + 0) = z4; *(u32x4*)(kL + 0 * KCS + 8) = z4;
            *(u32x4*)(kL + 1 * KCS + 0) = z4; *(u32x4*)(kL + 1 * KCS + 8) = e1;
            *(u32x4*)(kL + 2 * KCS + 0) = z4; *(u32x4*)(kL + 2 * KCS + 8) = e2;
            *(u32x4*)(kL + 3 * KCS + 0) = z4; *(u32x4*)(kL + 3 * KCS + 8) = e3;
        }
    }
    __syncthreads();
    if (T < 1620) {
        const u16* src = (tm < 2) ? ((tm ? w_up : w_gate) + (size_t)(tks * 32) * F + tcg * 64)
                                  : (w_down + (size_t)(tks * 32) * D + tcg * 64);
        const int ldm = (tm < 2) ? F : D;
        const u16* g0 = src + (size_t)(lane >> 3) * ldm + (lane & 7) * 8;
        const u16* g1 = g0 + (size_t)8 * ldm;
        const u16* g2 = g0 + (size_t)16 * ldm;
        const u16* g3 = g0 + (size_t)24 * ldm;
        const u32 l0 = __builtin_amdgcn_readfirstlane(lds_addr(tl));
        u32 keep;
        asm volatile(
            "s_mov_b32 %0, m0\n\t"
            "s_mov_b32 m0, %5\n\ts_nop 0\n\tglobal_load_lds_dwordx4 %1, off\n\t"
            "s_add_u32 m0, %5, 0x400\n\ts_nop 0\n\tglobal_load_lds_dwordx4 %2, off\n\t"
            "s_add_u32 m0, %5, 0x800\n\ts_nop 0\n\tglobal_load_lds_dwordx4 %3, off\n\t"
            "s_add_u32 m0, %5, 0xc00\n\ts_nop 0\n\tglobal_load_lds_dwordx4 %4, off\n\t"
            "s_mov_b32 m0, %0"
            : "=&s"(keep)
            : "v"(g0), "v"(g1), "v"(g2), "v"(g3), "s"(l0)
            : "memory", "scc");
    }
    const int li = lane & 15, g = lane >> 4;
    const u16* aBase = kL + ((li + 1) & 3) * KCS + ((li + 1) & ~3) + 8 * g + 64 * w;
    const u16* bBase = uL + UPAD + 16 * li + 8 - 8 * g - 64 * w;
    f32x4 acc[8];
#pragma unroll
    for (int c = 0; c < 8; ++c) acc[c] = (f32x4){0.f, 0.f, 0.f, 0.f};
    const u32 aAddr = lds_addr(aBase);
    const u32 bAddr = lds_addr(bBase - 32);
    s16x4 alo[2][8], ahi[2][8];
    bf16x8 bf[2][8];
#define LOAD_FRAGS(r)                                                                                   \
    _Pragma("unroll") for (int n = 0; n < 8; ++n) {                                                     \
        asm volatile("ds_read_b64 %0, %1 offset:%2" : "=v"(alo[r][n]) : "v"(aAddr), "i"(64 * (8 * n + r))); \
        asm volatile("ds_read_b64 %0, %1 offset:%2" : "=v"(ahi[r][n]) : "v"(aAddr), "i"(64 * (8 * n + r) + 8)); \
        asm volatile("ds_read_b128 %0, %1 offset:%2" : "=v"(bf[r][n]) : "v"(bAddr), "i"(64 * (8 * n - r) + 64)); \
    }
#define MFMA_ALL(r)                                                                                     \
    _Pragma("unroll") for (int sum = 0; sum < 8; ++sum) _Pragma("unroll") for (int n = 0; n <= sum; ++n) \
        acc[sum] = __builtin_amdgcn_mfma_f32_16x16x32_bf16(                                             \
            __builtin_shufflevector(alo[r][n], ahi[r][n], 0, 1, 2, 3, 4, 5, 6, 7), bf[r][sum - n], acc[sum], 0, 0, 0);
    LOAD_FRAGS(0)
    asm volatile("s_waitcnt lgkmcnt(0)" ::: "memory");
    __builtin_amdgcn_sched_barrier(0);
    LOAD_FRAGS(1)
    __builtin_amdgcn_sched_barrier(0);
    MFMA_ALL(0)
    __builtin_amdgcn_sched_barrier(0);
    asm volatile("s_waitcnt lgkmcnt(0)" ::: "memory");
    __builtin_amdgcn_sched_barrier(0);
    MFMA_ALL(1)
#undef LOAD_FRAGS
#undef MFMA_ALL
    __syncthreads();
#pragma unroll
    for (int c = 0; c < 8; ++c) {
        const int o = c >> 1;
        if (o != w) part[((o * 3 + ((w - o - 1) & 3)) * 2 + (c & 1)) * 64 + lane] = acc[c];
    }
    __syncthreads();
    if (T < 1620) {
        asm volatile("s_waitcnt vmcnt(0)" ::: "memory");
#pragma unroll
        for (int j = 0; j < 4; ++j) {
            const u16* rp = tl + (8 * g + (li >> 2)) * 64 + 16 * j + 4 * (li & 3);
            s16x4 b0 = tr_read(rp), b1 = tr_read(rp + 4 * 64);
            bf16x8 fr = __builtin_shufflevector(b0, b1, 0, 1, 2, 3, 4, 5, 6, 7);
            u16* dst;
            if (tm < 2) dst = (tm ? wuF : wgF) + ((size_t)((tcg * 4 + j) * 20 + tks) * 64 + lane) * 8;
            else dst = wdF + ((size_t)((tcg * 8 + 2 * j + (li >> 3)) * 54 + tks) * 32 + g * 8 + (li & 7)) * 8;
            asm volatile("global_store_dwordx4 %0, %1, off sc1\n\ts_nop 1" ::"v"(dst), "v"(fr) : "memory");
        }
    }
    u16* yrow = y2T + (size_t)d * S;
    u32x2 ov[2];
#pragma unroll
    for (int j = 0; j < 2; ++j) {
        const int c = 2 * w + j;
        f32x4 own = acc[j];
#pragma unroll
        for (int q = 1; q < 4; ++q) own = (w == q) ? acc[2 * q + j] : own;
        f32x4 a0 = part[((w * 3 + 0) * 2 + j) * 64 + lane], a1 = part[((w * 3 + 1) * 2 + j) * 64 + lane];
        f32x4 a2 = part[((w * 3 + 2) * 2 + j) * 64 + lane];
        f32x4 a = (a0 + a1) + (a2 + own);
        const int tb = 256 * c + 16 * li + 8 * (g >> 1);
        u32x2 uu = *(const u32x2*)(uL + UPAD + tb + 4 * (1 - (g & 1)));
        float u3 = lo2f(uu.x), u2 = hi2f(uu.x), u1 = lo2f(uu.y), u0 = hi2f(uu.y);
        u32 y01 = pk2(a[0], a[1]);
        u32 y23 = pk2(a[2], a[3]);
        u32 w01 = pk2(u0 * Dv, u1 * Dv);
        u32 w23 = pk2(u2 * Dv, u3 * Dv);
        ov[j].x = pk2(lo2f(y01) + lo2f(w01), hi2f(y01) + hi2f(w01));
        ov[j].y = pk2(lo2f(y23) + lo2f(w23), hi2f(y23) + hi2f(w23));
    }
    {
        auto rx = __builtin_amdgcn_permlane16_swap(ov[0].x, ov[1].x, false, false);
        auto ry = __builtin_amdgcn_permlane16_swap(ov[0].y, ov[1].y, false, false);
        u32x4 o = {rx[0], ry[0], rx[1], ry[1]};
        st16(yrow + 256 * (2 * w + (g & 1)) + 16 * li + 8 * (g >> 1), o);
    }
    asm volatile("s_waitcnt vmcnt(0)" ::: "memory");
}

__device__ __forceinline__ void unpack10(const u32x4& a, u32 b, float* f) {
    f[0] = lo2f(a.x); f[1] = hi2f(a.x); f[2] = lo2f(a.y); f[3] = hi2f(a.y);
    f[4] = lo2f(a.z); f[5] = hi2f(a.z); f[6] = lo2f(a.w); f[7] = hi2f(a.w);
    f[8] = lo2f(b); f[9] = hi2f(b);
}
__device__ __forceinline__ void ln_stats(const float* x, float& m_bf, float& s_bf) {
    float s = ((x[0] + x[1]) + (x[2] + x[3])) + ((x[4] + x[5]) + (x[6] + x[7])) + (x[8] + x[9]);
    s = wave_sum(s);
    const float mean = s * (1.0f / D);
    float q0 = 0.f, q1 = 0.f;
#pragma unroll
    for (int i = 0; i < 10; i += 2) {
        float d0 = x[i] - mean, d1 = x[i + 1] - mean;
        q0 += d0 * d0;
        q1 += d1 * d1;
    }
    float q = wave_sum(q0 + q1);
    const float var = q * (1.0f / D);
    m_bf = rbf(mean);
    float vb = rbf(var);
    float ve = rbf(vb + 1e-5f);
    s_bf = rbf(sqrtf(ve));
}

__global__ __launch_bounds__(512) void k2_row(const u16* __restrict__ q, const u16* __restrict__ y2T,
                                              const u16* __restrict__ w_r, u16* __restrict__ out,
                                              u16* __restrict__ hbuf, float* __restrict__ stats,
                                              u32* __restrict__ keys) {
    __shared__ __attribute__((aligned(16))) u32 yt[8][320];
    const int b = blockIdx.x;
    const int tb = (b & 7) * 32 + (b >> 3);
    const int t0 = tb * 8;
    const int tid = threadIdx.x;
    const int w = tid >> 6, lane = tid & 63;
    const int t = t0 + w;
    const u32x4 xa = *(const u32x4*)(q + (size_t)t * D + 8 * lane);
    const u32 xb = *(const u32*)(q + (size_t)t * D + 512 + 2 * lane);
    u32x4 ya = {0u, 0u, 0u, 0u}, yb = {0u, 0u, 0u, 0u};
    if (tid < 320) {
        ya = *(const u32x4*)(y2T + (size_t)(2 * tid) * S + t0);
        yb = *(const u32x4*)(y2T + (size_t)(2 * tid + 1) * S + t0);
    }
    const u32x4 wa = *(const u32x4*)(w_r + 8 * lane);
    const u32 wb = *(const u32*)(w_r + 512 + 2 * lane);
    float x[10];
    unpack10(xa, xb, x);
    float m1, s1;
    ln_stats(x, m1, s1);
    const float r1 = 1.0f / s1;
    if (tid < 320) {
        yt[0][tid] = __builtin_amdgcn_perm(yb.x, ya.x, 0x05040100);
        yt[1][tid] = __builtin_amdgcn_perm(yb.x, ya.x, 0x07060302);
        yt[2][tid] = __builtin_amdgcn_perm(yb.y, ya.y, 0x05040100);
        yt[3][tid] = __builtin_amdgcn_perm(yb.y, ya.y, 0x07060302);
        yt[4][tid] = __builtin_amdgcn_perm(yb.z, ya.z, 0x05040100);
        yt[5][tid] = __builtin_amdgcn_perm(yb.z, ya.z, 0x07060302);
        yt[6][tid] = __builtin_amdgcn_perm(yb.w, ya.w, 0x05040100);
        yt[7][tid] = __builtin_amdgcn_perm(yb.w, ya.w, 0x07060302);
    }
    u32 l1p[5];
#pragma unroll
    for (int i = 0; i < 10; i += 2) {
        u32 dd = pk2(x[i] - m1, x[i + 1] - m1);
        l1p[i >> 1] = pk2(lo2f(dd) * r1, hi2f(dd) * r1);
    }
    __syncthreads();
    const u32x4 y4 = *(const u32x4*)(&yt[w][4 * lane]);
    const u32 y1 = yt[w][256 + lane];
    float y[10];
    unpack10(y4, y1, y);
    float h[10];
    u32 hp[5], op[5];
#pragma unroll
    for (int i = 0; i < 10; i += 2) {
        u32 l1 = l1p[i >> 1];
        u32 a = pk2(lo2f(l1) * y[i], hi2f(l1) * y[i + 1]);
        u32 hh = pk2(x[i] + lo2f(a), x[i + 1] + hi2f(a));
        hp[i >> 1] = hh;
        h[i] = lo2f(hh);
        h[i + 1] = hi2f(hh);
    }
    st16(hbuf + (size_t)t * D + 8 * lane, (u32x4){hp[0], hp[1], hp[2], hp[3]});
    {
        u32x4 tq = {(u32)__builtin_amdgcn_mov_dpp((int)hp[4], 0x00, 0xf, 0xf, true), (u32)__builtin_amdgcn_mov_dpp((int)hp[4], 0x55, 0xf, 0xf, true),
                    (u32)__builtin_amdgcn_mov_dpp((int)hp[4], 0xaa, 0xf, 0xf, true), (u32)__builtin_amdgcn_mov_dpp((int)hp[4], 0xff, 0xf, 0xf, true)};
        if ((lane & 3) == 0) st16(hbuf + (size_t)t * D + 512 + 2 * lane, tq);
    }
    float m2, s2;
    ln_stats(h, m2, s2);
    const float r2 = 1.0f / s2;
    float wr[10];
    unpack10(wa, wb, wr);
    float racc0 = 0.f, racc1 = 0.f;
#pragma unroll
    for (int i = 0; i < 10; i += 2) {
        u32 dd = pk2(h[i] - m2, h[i + 1] - m2);
        u32 l2 = pk2(lo2f(dd) * r2, hi2f(dd) * r2);
        racc0 += lo2f(l2) * wr[i];
        racc1 += hi2f(l2) * wr[i + 1];
        op[i >> 1] = pk2(h[i] + lo2f(l2), h[i + 1] + hi2f(l2));
    }
    st16(out + (size_t)t * D + 8 * lane, (u32x4){op[0], op[1], op[2], op[3]});
    {
        u32x4 tq = {(u32)__builtin_amdgcn_mov_dpp((int)op[4], 0x00, 0xf, 0xf, true), (u32)__builtin_amdgcn_mov_dpp((int)op[4], 0x55, 0xf, 0xf, true),
                    (u32)__builtin_amdgcn_mov_dpp((int)op[4], 0xaa, 0xf, 0xf, true), (u32)__builtin_amdgcn_mov_dpp((int)op[4], 0xff, 0xf, 0xf, true)};
        if ((lane & 3) == 0) st16(out + (size_t)t * D + 512 + 2 * lane, tq);
    }
    const float racc = wave_sum(racc0 + racc1);
    if (lane == 0) {
        u32 rb = pk2(racc, 0.f) & 0xffffu;
        u32 ord = (rb & 0x8000u) ? (rb ^ 0xffffu) : (rb ^ 0x8000u);
        keys[t] = (ord << 16) | (u32)(0xffff - t);
        *(f32x2*)(stats + 2 * t) = (f32x2){m2, r2};
    }
}

__device__ __forceinline__ float key_r(u32 key) {
    u32 ord = key >> 16;
    u32 rb = (ord & 0x8000u) ? (ord ^ 0x8000u) : (ord ^ 0xffffu);
    return __uint_as_float(rb << 16);
}

constexpr int XS = 648;
__global__ __launch_bounds__(512) void k3_gateup(const u32* __restrict__ keys, const u16* __restrict__ hbuf,
                                                 const float* __restrict__ stats, const u16* __restrict__ wgF,
                                                 const u16* __restrict__ wuF, u16* __restrict__ gu,
                                                 u32* __restrict__ selkeys, u16* __restrict__ selh,
                                                 u16* __restrict__ sell2) {
    __shared__ __attribute__((aligned(16))) u16 xs[8 * XS];
    __shared__ __attribute__((aligned(16))) float red[8][2][8][16];
    __shared__ u32 cand[64];
    const int b = blockIdx.x;
    const int slice = b;
    const int f0 = slice * 16;
    const int tid = threadIdx.x, lane = tid & 63;
    const int w = __builtin_amdgcn_readfirstlane(tid >> 6);
    u32 a[4];
    {
        u32x4 k0 = *(const u32x4*)(keys + tid * 4);
        a[0] = k0.x; a[1] = k0.y; a[2] = k0.z; a[3] = k0.w;
    }
    bf16x8 bg[3], bu[3];
#pragma unroll
    for (int i = 0; i < 3; ++i) {
        const int ks = (w + 8 * i < 20) ? (w + 8 * i) : w;
        bg[i] = *(const bf16x8*)(wgF + ((size_t)(slice * 20 + ks) * 64 + lane) * 8);
        bu[i] = *(const bf16x8*)(wuF + ((size_t)(slice * 20 + ks) * 64 + lane) * 8);
    }
#define CE(i, j) { u32 hi_ = a[i] > a[j] ? a[i] : a[j]; u32 lo_ = a[i] > a[j] ? a[j] : a[i]; a[i] = hi_; a[j] = lo_; }
    CE(0, 1) CE(2, 3) CE(0, 2) CE(1, 3) CE(1, 2)
#undef CE
    u32 mine = 0;
#pragma unroll
    for (int rd = 0; rd < 8; ++rd) {
        u32 m = wave_max_u32(a[0]);
        bool win = (a[0] == m);
        a[0] = win ? a[1] : a[0];
        a[1] = win ? a[2] : a[1];
        a[2] = win ? a[3] : a[2];
        a[3] = win ? 0u : a[3];
        if (lane == rd) mine = m;
    }
    if (lane < 8) cand[w * 8 + lane] = mine;
    __syncthreads();
    {
        u32 c = cand[lane];
        u32 key = 0;
        for (int rd = 0; rd <= w; ++rd) {
            key = wave_max_u32(c);
            c = (c == key) ? 0u : c;
        }
        const int ta = 0xffff - (int)(key & 0xffffu);
        const u32x4 ha = *(const u32x4*)(hbuf + (size_t)ta * D + 8 * lane);
        const u32 hb = *(const u32*)(hbuf + (size_t)ta * D + 512 + 2 * lane);
        const f32x2 st0 = *(const f32x2*)(stats + 2 * ta);
        const float m2 = st0.x, r2 = st0.y;
        float h[10];
        unpack10(ha, hb, h);
        u32 l2[5];
#pragma unroll
        for (int i = 0; i < 10; i += 2) {
            u32 dd = pk2(h[i] - m2, h[i + 1] - m2);
            l2[i >> 1] = pk2(lo2f(dd) * r2, hi2f(dd) * r2);
        }
        *(u32x4*)(xs + w * XS + 8 * lane) = (u32x4){l2[0], l2[1], l2[2], l2[3]};
        *(u32*)(xs + w * XS + 512 + 2 * lane) = l2[4];
        if (b == 0) {
            *(u32x4*)(selh + w * D + 8 * lane) = ha;
            *(u32*)(selh + w * D + 512 + 2 * lane) = hb;
            *(u32x4*)(sell2 + w * D + 8 * lane) = (u32x4){l2[0], l2[1], l2[2], l2[3]};
            *(u32*)(sell2 + w * D + 512 + 2 * lane) = l2[4];
            if (lane == 0) selkeys[w] = key;
        }
    }
    __syncthreads();
    {
        const int li = lane & 15, g = lane >> 4;
        f32x4 ag = {0.f, 0.f, 0.f, 0.f}, au = {0.f, 0.f, 0.f, 0.f};
#pragma unroll
        for (int s = 0; s < 2; ++s) {
            const int k0 = (w + 8 * s) * 32;
            bf16x8 af = *(const bf16x8*)(xs + (li & 7) * XS + k0 + 8 * g);
            ag = __builtin_amdgcn_mfma_f32_16x16x32_bf16(af, bg[s], ag, 0, 0, 0);
            au = __builtin_amdgcn_mfma_f32_16x16x32_bf16(af, bu[s], au, 0, 0, 0);
        }
        {
            const int k0 = ((w < 4) ? (w + 16) : w) * 32;
            bf16x8 af = *(const bf16x8*)(xs + (li & 7) * XS + k0 + 8 * g);
            f32x4 ag2 = __builtin_amdgcn_mfma_f32_16x16x32_bf16(af, bg[2], ag, 0, 0, 0);
            f32x4 au2 = __builtin_amdgcn_mfma_f32_16x16x32_bf16(af, bu[2], au, 0, 0, 0);
            if (w < 4) { ag = ag2; au = au2; }
        }
        if (g < 2) {
#pragma unroll
            for (int rr = 0; rr < 4; ++rr) {
                red[w][0][4 * g + rr][li] = ag[rr];
                red[w][1][4 * g + rr][li] = au[rr];
            }
        }
    }
    __syncthreads();
    if (tid < 128) {
        const int tok = tid >> 4, col = tid & 15;
        float gs = ((red[0][0][tok][col] + red[1][0][tok][col]) + (red[2][0][tok][col] + red[3][0][tok][col])) +
                   ((red[4][0][tok][col] + red[5][0][tok][col]) + (red[6][0][tok][col] + red[7][0][tok][col]));
        float us = ((red[0][1][tok][col] + red[1][1][tok][col]) + (red[2][1][tok][col] + red[3][1][tok][col])) +
                   ((red[4][1][tok][col] + red[5][1][tok][col]) + (red[6][1][tok][col] + red[7][1][tok][col]));
        float gb = rbf(gs), ub = rbf(us);
        float sg = rbf(gb / (1.0f + __expf(-gb)));
        u32 o = pk2(sg * ub, 0.f);
        const int f = f0 + col;
        gu[((f >> 5) * 4 + ((f >> 3) & 3)) * 64 + tok * 8 + (f & 7)] = (u16)(o & 0xffffu);
    }
}

__global__ __launch_bounds__(512) void k4_down(const u16* __restrict__ gu, const u16* __restrict__ wdF,
                                               const u32* __restrict__ selkeys, const u16* __restrict__ selh,
                                               const u16* __restrict__ sell2, u16* __restrict__ out) {
    __shared__ __attribute__((aligned(16))) float red[8][8][16];
    const int slice = blockIdx.x;
    const int d0 = slice * 8;
    const int tid = threadIdx.x, lane = tid & 63;
    const int w = __builtin_amdgcn_readfirstlane(tid >> 6);
    const int li = lane & 15, g = lane >> 4;
    bf16x8 af[7], bf[7];
    {
        const u16* pa = gu + (w * 4 + g) * 64 + (li & 7) * 8;
        const u16* pb = wdF + ((size_t)(slice * 54 + w) * 32 + g * 8 + (li & 7)) * 8;
#pragma unroll
        for (int j = 0; j < 7; ++j) {
            const int jj = (j == 6 && w >= 6) ? 5 : j;
            af[j] = *(const bf16x8*)(pa + jj * 8 * 256);
            bf[j] = *(const bf16x8*)(pb + jj * 8 * 256);
        }
    }
    const int tok_ = (tid >> 3) & 7, col_ = tid & 7;
    const u32 key = selkeys[tok_];
    const float hv = __uint_as_float(((u32)selh[tok_ * D + d0 + col_]) << 16);
    const float l2v = __uint_as_float(((u32)sell2[tok_ * D + d0 + col_]) << 16);
    __builtin_amdgcn_sched_barrier(0);
    {
        f32x4 acc = {0.f, 0.f, 0.f, 0.f};
#pragma unroll
        for (int j = 0; j < 6; ++j) acc = __builtin_amdgcn_mfma_f32_16x16x32_bf16(af[j], bf[j], acc, 0, 0, 0);
        f32x4 acc2 = __builtin_amdgcn_mfma_f32_16x16x32_bf16(af[6], bf[6], acc, 0, 0, 0);
        if (w < 6) acc = acc2;
        if (g < 2) {
#pragma unroll
            for (int rr = 0; rr < 4; ++rr) red[w][4 * g + rr][li] = acc[rr];
        }
    }
    __syncthreads();
    if (tid < 64) {
        const int tok = tid >> 3, col = tid & 7;
        const int trow = 0xffff - (int)(key & 0xffffu);
        const float rv = key_r(key);
        const float rs = rbf(1.0f / (1.0f + __expf(-rv)));
        float s = ((red[0][tok][col] + red[1][tok][col]) + (red[2][tok][col] + red[3][tok][col])) +
                  ((red[4][tok][col] + red[5][tok][col]) + (red[6][tok][col] + red[7][tok][col]));
        float y = rbf(s);
        y = rbf(y * rs);
        float o1 = rbf(l2v + y);
        u32 o = pk2(hv + o1, 0.f);
        out[(size_t)trow * D + d0 + col] = (u16)(o & 0xffffu);
    }
}

extern "C" void kernel_launch(void* const* d_in, const int* in_sizes, int n_in, void* d_out, int out_size,
                              void* d_ws, size_t ws_size, hipStream_t stream) {
    const u16* q = (const u16*)d_in[0];
    const u16* k = (const u16*)d_in[1];
    const u16* v = (const u16*)d_in[2];
    const u16* shift_k = (const u16*)d_in[3];
    const u16* ssm_k = (const u16*)d_in[4];
    const u16* ssm_D = (const u16*)d_in[5];
    const u16* w_r = (const u16*)d_in[7];
    const u16* w_gate = (const u16*)d_in[8];
    const u16* w_up = (const u16*)d_in[9];
    const u16* w_down = (const u16*)d_in[10];
    u16* out = (u16*)d_out;
    char* ws = (char*)d_ws;
    const size_t TS = (size_t)S * D * 2;
    u16* uT = (u16*)(ws);
    u16* y2T = (u16*)(ws + TS);
    u16* hbuf = (u16*)(ws);
    char* sm = ws + 2 * TS;
    float* stats = (float*)(sm);
    u32* keys = (u32*)(sm + 16384);
    u32* selkeys = (u32*)(sm + 24576);
    u16* selh = (u16*)(sm + 24576 + 256);
    u16* sell2 = (u16*)(sm + 24576 + 256 + 10240);
    u16* gu = (u16*)(sm + 24576 + 256 + 20480);
    const size_t WS_ = (size_t)D * F * 2;
    u16* wgF = (u16*)(sm + 131072);
    u16* wuF = (u16*)(sm + 131072 + WS_);
    u16* wdF = (u16*)(sm + 131072 + 2 * WS_);
    k0_kv<<<256, 640, 0, stream>>>(k, v, shift_k, uT, ssm_k, q);
    k1_conv<<<D, 256, 0, stream>>>(uT, ssm_k, ssm_D, y2T, w_gate, w_up, w_down, wgF, wuF, wdF);
    k2_row<<<256, 512, 0, stream>>>(q, y2T, w_r, out, hbuf, stats, keys);
    k3_gateup<<<108, 512, 0, stream>>>(keys, hbuf, stats, wgF, wuF, gu, selkeys, selh, sell2);
    k4_down<<<80, 512, 0, stream>>>(gu, wdF, selkeys, selh, sell2, out);
}
```
